```python
import torch

def _softmax(x, axis=-1):
    return torch.softmax(x, dim=axis)

def _gelu(x):
    return torch.nn.functional.gelu(x, approximate='tanh')

def _silu(x):
    return torch.nn.functional.silu(x)

def _sigmoid(x):
    return torch.sigmoid(x)

def _layernorm(x, axis=-1, eps=1e-05):
    m = x.mean(dim=axis, keepdim=True)
    v = x.var(dim=axis, keepdim=True, unbiased=False)
    return (x - m) / torch.sqrt(v + eps)

def _scatter_add(tgt, idx, src, axis=0):
    return tgt.clone().index_add_(axis, idx, src)

def _segment_sum(src, seg, n):
    out = torch.zeros((n,) + src.shape[1:], dtype=src.dtype)
    return out.index_add_(0, seg, src)

def _pad(x, pw):
    flat = tuple((v for pair in reversed(pw) for v in pair))
    return torch.nn.functional.pad(x, flat)
kernel_name = 'adaLN_cond__gqa_block_cached__swiglu_ffn__decode'

def setup_inputs():

    _w = lambda a: (lambda t: t.to(torch.bfloat16) if t.is_floating_point() else t)(torch.as_tensor(a))

    def _A_setup():
        B, Sq, D = (4, 1, 512)
        Hq, Hkv, Sk, Dh = (32, 8, 4096, 128)
        assert Hq % Hkv == 0, (Hq, Hkv)
        return {'x': _w((lambda *s: torch.randn(*s, dtype=torch.bfloat16) * 0.05)(B, Sq, D)), 'k_cache': _w((lambda *s: torch.randn(*s, dtype=torch.bfloat16) * 3.2)(B, Hkv, Sk, Dh)), 'v_cache': _w((lambda *s: torch.randn(*s, dtype=torch.bfloat16) * 0.4)(B, Hkv, Sk, Dh)), 'wq': _w((lambda *s: torch.randn(*s, dtype=torch.bfloat16) * 3.2)(D, Hq * Dh) * D ** (-0.5)), 'wo': _w((lambda *s: torch.randn(*s, dtype=torch.bfloat16) * 0.4)(Hq * Dh, D) * (Hq * Dh) ** (-0.5))}
    _A_in = _A_setup()

    def _B_setup():
        B, S, D, F = (4, 1, 512, 1792)
        return {'x': _w((lambda *s: torch.randn(*s, dtype=torch.bfloat16) * 0.05)(B, S, D)), 'w_gate': _w((lambda *s: torch.randn(*s, dtype=torch.bfloat16) * 0.05)(D, F)), 'w_up': _w((lambda *s: torch.randn(*s, dtype=torch.bfloat16) * 0.05)(D, F)), 'w_down': _w((lambda *s: torch.randn(*s, dtype=torch.bfloat16) * 0.05)(F, D))}
    _B_in = _B_setup()
    return {'A_x': _A_in['x'], 'A_k_cache': _A_in['k_cache'], 'A_v_cache': _A_in['v_cache'], 'A_wq': _A_in['wq'], 'A_wo': _A_in['wo'], 'B_x': _B_in['x'], 'B_w_gate': _B_in['w_gate'], 'B_w_up': _B_in['w_up'], 'B_w_down': _B_in['w_down'], 'c': _w((lambda *s: torch.randn(*s, dtype=torch.bfloat16) * 0.2)(4, 512) * 8.0), 'w_mod': _w((lambda *s: torch.randn(*s, dtype=torch.bfloat16) * 0.2)(512, 6 * 512) * 512 ** (-0.5) * 8.0)}

def reference(A_x, A_k_cache, A_v_cache, A_wq, A_wo, B_x, B_w_gate, B_w_up, B_w_down, c, w_mod):

    def _A_ref(x, k_cache, v_cache, wq, wo):
        B, Sq, D = x.shape
        Hq, Dh = (32, 128)
        Hkv = k_cache.shape[1]
        rep = Hq // Hkv
        q = (lambda x, *d: x.permute(*d))((x @ wq).reshape(B, Sq, Hq, Dh), 0, 2, 1, 3)
        k = (lambda x, n, axis: torch.repeat_interleave(x, n, dim=axis))(k_cache, rep, axis=1)
        v = (lambda x, n, axis: torch.repeat_interleave(x, n, dim=axis))(v_cache, rep, axis=1)
        scores = torch.einsum('bhqd,bhkd->bhqk', q, k) * Dh ** (-0.5)
        p = _softmax(scores, axis=-1)
        o = torch.einsum('bhqk,bhkd->bhqd', p, v)
        return (lambda x, *d: x.permute(*d))(o, 0, 2, 1, 3).reshape(B, Sq, Hq * Dh) @ wo

    def _B_ref(x, w_gate, w_up, w_down):
        g = _silu(x @ w_gate)
        u = x @ w_up
        return g * u @ w_down
    x = A_x
    mod = (_silu(c) @ w_mod).reshape(c.shape[0], 6, 1, -1)
    sa, ca, ga, sb, cb, gb = (mod[:, 0].clone(), mod[:, 1].clone(), mod[:, 2].clone(), mod[:, 3].clone(), mod[:, 4].clone(), mod[:, 5].clone())
    h = _layernorm(x, axis=-1) * (1 + ca) + sa
    x = x + ga * _A_ref(h, A_k_cache, A_v_cache, A_wq, A_wo)
    h = _layernorm(x, axis=-1) * (1 + cb) + sb
    x = x + gb * _B_ref(h, B_w_gate, B_w_up, B_w_down)
    return x
```

```cpp
#include <hip/hip_runtime.h>
#include <hip/hip_bf16.h>
#include <stdint.h>

#define MIMIC 0

typedef unsigned short u16;
__device__ __forceinline__ float bf2f(u16 b) { return __builtin_bit_cast(float, (unsigned)b << 16); }
__device__ __forceinline__ u16 f2bf(float f) { unsigned u = __builtin_bit_cast(unsigned, f); u += 0x7FFFu + ((u >> 16) & 1u); return (u16)(u >> 16); }
__device__ __forceinline__ float rb(float f) {
  return f;
}

constexpr int B = 4, D = 512, HQ = 32, HKV = 8, SK = 4096, DH = 128, F = 1792;

constexpr int WS_MOD = 0;
constexpr int WS_Q = WS_MOD + 4 * 3072;
constexpr int WS_S = WS_Q + 4 * 4096;
constexpr int WS_O = WS_S + 4 * 32 * 4096;
constexpr int WS_X1 = WS_O + 4 * 4096;
constexpr int WS_ACT = WS_X1 + 4 * 512;

__global__ void k_mod(const u16* c, const u16* wmod, float* ws) {
  __shared__ float sc[4][512];
  for (int i = threadIdx.x; i < 2048; i += blockDim.x) { float v = bf2f(c[i]); sc[i / 512][i % 512] = rb(v / (1.f + __expf(-v))); }
  __syncthreads();
  int n = blockIdx.x * blockDim.x + threadIdx.x;
  float a[4] = {0, 0, 0, 0};
  for (int k = 0; k < 512; ++k) { float w = bf2f(wmod[k * 3072 + n]); for (int b = 0; b < 4; ++b) a[b] += sc[b][k] * w; }
  for (int b = 0; b < 4; ++b) ws[WS_MOD + b * 3072 + n] = rb(a[b]);
}

__device__ void ln_mod(const float* x  , float* h  , const float* shift, const float* scale  ) {
  int w = threadIdx.x / 64, lane = threadIdx.x % 64;
  float s = 0;
  for (int i = lane; i < 512; i += 64) s += x[w * 512 + i];
  for (int o = 32; o; o >>= 1) s += __shfl_xor(s, o);
  float mean = s / 512.f;
  float v = 0;
  for (int i = lane; i < 512; i += 64) { float d = x[w * 512 + i] - mean; v += d * d; }
  for (int o = 32; o; o >>= 1) v += __shfl_xor(v, o);
  v = rb(v / 512.f);
  float m = rb(mean);
  float den = rb(sqrtf(rb(v + 1e-5f)));
  for (int i = lane; i < 512; i += 64) {
    float n = rb(rb(x[w * 512 + i] - m) / den);
    float t = rb(n * rb(1.f + scale[w * 3072 + i]));
    h[w * 512 + i] = rb(t + shift[w * 3072 + i]);
  }
}

__global__ void k_q(const u16* x, const u16* wq, float* ws) {
  __shared__ float sx[2048], sh[2048];
  for (int i = threadIdx.x; i < 2048; i += blockDim.x) sx[i] = bf2f(x[i]);
  __syncthreads();
  ln_mod(sx, sh, ws + WS_MOD + 0, ws + WS_MOD + 512);
  __syncthreads();
  int n = blockIdx.x * blockDim.x + threadIdx.x;
  float a[4] = {0, 0, 0, 0};
  for (int k = 0; k < 512; ++k) { float w = bf2f(wq[k * 4096 + n]); for (int b = 0; b < 4; ++b) a[b] += sh[b * 512 + k] * w; }
  for (int b = 0; b < 4; ++b) ws[WS_Q + b * 4096 + n] = rb(a[b]);
}

__global__ void k_scores(const u16* kc, float* ws) {
  __shared__ float sq[4][128];
  int b = blockIdx.z, hk = blockIdx.y, key = blockIdx.x * 256 + threadIdx.x;
  for (int i = threadIdx.x; i < 512; i += 256) sq[i / 128][i % 128] = ws[WS_Q + b * 4096 + hk * 512 + i];
  __syncthreads();
  const u16* kr = kc + ((size_t)(b * HKV + hk) * SK + key) * DH;
  float a[4] = {0, 0, 0, 0};
  for (int d = 0; d < 128; ++d) { float kv = bf2f(kr[d]); for (int g = 0; g < 4; ++g) a[g] += sq[g][d] * kv; }
  for (int g = 0; g < 4; ++g) ws[WS_S + (size_t)(b * 32 + hk * 4 + g) * SK + key] = rb(rb(a[g]) * 0.08838834764831845f);
}

__global__ void k_pv(const u16* vc, float* ws) {
  __shared__ float sp[4096];
  __shared__ float red[256];
  int h = blockIdx.x, b = blockIdx.y, t = threadIdx.x;
  const float* s = ws + WS_S + (size_t)(b * 32 + h) * SK;
  float m = -INFINITY;
  for (int i = t; i < SK; i += 256) m = fmaxf(m, s[i]);
  red[t] = m; __syncthreads();
  for (int o = 128; o; o >>= 1) { if (t < o) red[t] = fmaxf(red[t], red[t + o]); __syncthreads(); }
  m = red[0]; __syncthreads();
  float l = 0;
  for (int i = t; i < SK; i += 256) { float e = __expf(s[i] - m); sp[i] = e; l += e; }
  red[t] = l; __syncthreads();
  for (int o = 128; o; o >>= 1) { if (t < o) red[t] += red[t + o]; __syncthreads(); }
  l = red[0]; __syncthreads();
  for (int i = t; i < SK; i += 256) sp[i] = rb(sp[i] / l);
  __syncthreads();
  int d = t % 128, half = t / 128;
  const u16* vr = vc + (size_t)(b * HKV + h / 4) * SK * DH;
  float a = 0;
  for (int k = half * 2048; k < half * 2048 + 2048; ++k) a += sp[k] * bf2f(vr[(size_t)k * DH + d]);
  red[t] = a; __syncthreads();
  if (t < 128) ws[WS_O + b * 4096 + h * 128 + t] = rb(red[t] + red[t + 128]);
}

__global__ void k_wo(const u16* x, const u16* wo, float* ws) {
  __shared__ float red[4][256];
  int n = blockIdx.x * 64 + threadIdx.x % 64, kg = threadIdx.x / 64;
  float a[4] = {0, 0, 0, 0};
  for (int k = kg * 1024; k < kg * 1024 + 1024; ++k) { float w = bf2f(wo[k * 512 + n]); for (int b = 0; b < 4; ++b) a[b] += ws[WS_O + b * 4096 + k] * w; }
  for (int b = 0; b < 4; ++b) red[b][threadIdx.x] = a[b];
  __syncthreads();
  if (threadIdx.x < 64) for (int b = 0; b < 4; ++b) {
    float s = rb(red[b][threadIdx.x] + red[b][threadIdx.x + 64] + red[b][threadIdx.x + 128] + red[b][threadIdx.x + 192]);
    float ga = ws[WS_MOD + b * 3072 + 1024 + n];
    ws[WS_X1 + b * 512 + n] = rb(bf2f(x[b * 512 + n]) + rb(ga * s));
  }
}

__global__ void k_gu(const u16* wg, const u16* wu, float* ws) {
  __shared__ float sx[2048], sh[2048];
  for (int i = threadIdx.x; i < 2048; i += blockDim.x) sx[i] = ws[WS_X1 + i];
  __syncthreads();
  ln_mod(sx, sh, ws + WS_MOD + 1536, ws + WS_MOD + 2048);
  __syncthreads();
  int n = blockIdx.x * blockDim.x + threadIdx.x;
  float a[4] = {0, 0, 0, 0}, u[4] = {0, 0, 0, 0};
  for (int k = 0; k < 512; ++k) { float w = bf2f(wg[k * F + n]), w2 = bf2f(wu[k * F + n]); for (int b = 0; b < 4; ++b) { a[b] += sh[b * 512 + k] * w; u[b] += sh[b * 512 + k] * w2; } }
  for (int b = 0; b < 4; ++b) { float g = rb(a[b]); g = rb(g / (1.f + __expf(-g))); ws[WS_ACT + b * F + n] = rb(g * rb(u[b])); }
}

__global__ void k_down(const u16* wd, float* ws, u16* out) {
  __shared__ float red[4][256];
  int n = blockIdx.x * 64 + threadIdx.x % 64, kg = threadIdx.x / 64;
  float a[4] = {0, 0, 0, 0};
  for (int k = kg * 448; k < kg * 448 + 448; ++k) { float w = bf2f(wd[k * 512 + n]); for (int b = 0; b < 4; ++b) a[b] += ws[WS_ACT + b * F + k] * w; }
  for (int b = 0; b < 4; ++b) red[b][threadIdx.x] = a[b];
  __syncthreads();
  if (threadIdx.x < 64) for (int b = 0; b < 4; ++b) {
    float s = rb(red[b][threadIdx.x] + red[b][threadIdx.x + 64] + red[b][threadIdx.x + 128] + red[b][threadIdx.x + 192]);
    float gb = ws[WS_MOD + b * 3072 + 2560 + n];
    out[b * 512 + n] = f2bf(ws[WS_X1 + b * 512 + n] + rb(gb * s));
  }
}

extern "C" void kernel_launch(void* const* d_in, const int* in_sizes, int n_in, void* d_out, int out_size, void* d_ws, size_t ws_size, hipStream_t stream) {
  const u16* x = (const u16*)d_in[0]; const u16* kc = (const u16*)d_in[1]; const u16* vc = (const u16*)d_in[2];
  const u16* wq = (const u16*)d_in[3]; const u16* wo = (const u16*)d_in[4];
  const u16* wg = (const u16*)d_in[6]; const u16* wu = (const u16*)d_in[7]; const u16* wd = (const u16*)d_in[8];
  const u16* c = (const u16*)d_in[9]; const u16* wmod = (const u16*)d_in[10];
  float* ws = (float*)d_ws;
  k_mod<<<3072 / 256, 256, 0, stream>>>(c, wmod, ws);
  k_q<<<4096 / 256, 256, 0, stream>>>(x, wq, ws);
  k_scores<<<dim3(SK / 256, HKV, B), 256, 0, stream>>>(kc, ws);
  k_pv<<<dim3(32, 4), 256, 0, stream>>>(vc, ws);
  k_wo<<<8, 256, 0, stream>>>(x, wo, ws);
  k_gu<<<F / 256, 256, 0, stream>>>(wg, wu, ws);
  k_down<<<8, 256, 0, stream>>>(wd, ws, (u16*)d_out);
}
```

```cpp
#include <hip/hip_runtime.h>
#include <hip/hip_bf16.h>
#include <stdint.h>

typedef unsigned u32;
typedef unsigned short u16;
typedef unsigned long long u64;
typedef u32 v4u __attribute__((ext_vector_type(4)));
typedef u32 v2u __attribute__((ext_vector_type(2)));
typedef float v4f __attribute__((ext_vector_type(4)));
typedef float v2f __attribute__((ext_vector_type(2)));
typedef __bf16 v2b __attribute__((ext_vector_type(2)));
typedef short v8s __attribute__((ext_vector_type(8)));
#define LAS __attribute__((address_space(3)))
#define DEV __device__ __forceinline__

#define WIN 8
#define AW 8
#define D1 4
#define D2 8
#define D3 10
#define D4 10
#define D5 10
#define APRIO 3
#define WO_EARLY 0

constexpr int WS_EPOCH = 0;
constexpr int WS_H = 4096;
constexpr int WS_Q = WS_H + 65536;
constexpr int WS_ML = WS_Q + 32768;
constexpr int WS_OP = WS_ML + 32768;
constexpr int WS_MODL = WS_OP + 262144;
constexpr int WS_Y = WS_MODL + 262144;
constexpr int WS_Z = WS_Y + 524288;
constexpr int WS_END = WS_Z + 4 * 56 * 2048;
constexpr int WS_STAMP = 4 << 20;

constexpr int L_K = 0;
constexpr int L_P = 131072;
constexpr int L_OPW = L_P + 4096;
constexpr int L_FL = L_OPW + 8192;
constexpr int F_KDONE = L_FL, F_PDONE = L_FL + 16, F_OPUB = L_FL + 32, F_ABAR = L_FL + 48;
constexpr int L_MP = L_FL + 256;
constexpr int L_XST = L_MP + 2048;
constexpr int L_QP = L_XST + 64;
constexpr int L_MX = L_QP + 1024;
constexpr int L_LP = L_MX + 64;
constexpr int L_AP = L_LP + 4096;
constexpr int L_YP = L_AP + 2048;
constexpr int L_GP = L_YP + 1024;
constexpr int L_ST = L_GP + 1024;
constexpr int L_X1 = L_ST + 64;
constexpr int L_TOTAL = L_X1 + 2048;

constexpr u32 SENT = 0xFFFFFFFFu;
constexpr int GSPIN = 1 << 16, LSPIN = 1 << 20;

DEV float u2f(u32 u) { return __builtin_bit_cast(float, u); }
DEV u32 f2u(float f) { return __builtin_bit_cast(u32, f); }
DEV u32 cvtpk(float a, float b) { v2f f = {a, b}; return __builtin_bit_cast(u32, __builtin_convertvector(f, v2b)); }
DEV float blo(u32 p) { return u2f(p << 16); }
DEV float bhi(u32 p) { return u2f(p & 0xFFFF0000u); }
DEV float rb(float f) { return blo(cvtpk(f, f)); }
DEV u32 canon(u32 u) { return u < 0xFFFFFFFEu ? u : 0xFFFFFFFEu; }
DEV float ex2(float v) { return __builtin_amdgcn_exp2f(v); }
DEV float rcp(float v) { return __builtin_amdgcn_rcpf(v); }
constexpr float LOG2E = 1.4426950408889634f;
DEV float silu(float v) { return v * rcp(1.f + ex2(-LOG2E * v)); }
template <int C> DEV u32 dppu(u32 v) { return (u32)__builtin_amdgcn_mov_dpp((int)v, C, 0xf, 0xf, true); }
template <int C> DEV float dppf(float v) { return u2f(dppu<C>(f2u(v))); }
constexpr int XOR1 = 0xB1, XOR2 = 0x4E, ROR4 = 0x124, ROR8 = 0x128, SHL = 0x100, NBC = 0x150;
DEV void swap32(float& a, float& b) { auto r = __builtin_amdgcn_permlane32_swap(f2u(a), f2u(b), false, false); a = u2f(r[0]); b = u2f(r[1]); }
DEV void swap16(float& a, float& b) { auto r = __builtin_amdgcn_permlane16_swap(f2u(a), f2u(b), false, false); a = u2f(r[0]); b = u2f(r[1]); }
DEV float sum16(float v) { float a = v, b = v; swap16(a, b); return a + b; }
DEV float sum32(float v) { float a = v, b = v; swap32(a, b); return a + b; }
DEV float max16(float v) { float a = v, b = v; swap16(a, b); return fmaxf(a, b); }
DEV float max32(float v) { float a = v, b = v; swap32(a, b); return fmaxf(a, b); }
DEV float wsum(float v) { v += dppf<XOR1>(v); v += dppf<XOR2>(v); v += dppf<ROR4>(v); v += dppf<ROR8>(v); return sum32(sum16(v)); }
DEV v4f mfma(v4u a, v4u b, v4f c) { return __builtin_amdgcn_mfma_f32_16x16x32_bf16(__builtin_bit_cast(v8s, a), __builtin_bit_cast(v8s, b), c, 0, 0, 0); }
DEV u32 plo(u32 k0, u32 k1) { return __builtin_amdgcn_perm(k1, k0, 0x05040100u); }
DEV u32 phi(u32 k0, u32 k1) { return __builtin_amdgcn_perm(k1, k0, 0x07060302u); }
DEV float sel4(int g, float a, float b, float c, float d) { float lo = (g & 1) ? b : a, hi = (g & 1) ? d : c; return (g & 2) ? hi : lo; }
DEV u32 umax(u32 a, u32 b) { return a > b ? a : b; }
DEV u32 max4(v4u v) { return umax(umax(v.x, v.y), umax(v.z, v.w)); }
DEV float sum4(v4f v) { return (v[0] + v[1]) + (v[2] + v[3]); }
#define CBAR() asm volatile("" ::: "memory")
#define SCHED() __builtin_amdgcn_sched_barrier(0)
#define PIN(v) asm volatile("" : "+v"(v))
#define SPIN(N) _Pragma("unroll 1") for (int sp = 0; sp < (N); ++sp)

typedef __amdgpu_buffer_rsrc_t rsrc_t;
DEV v4u ld16(rsrc_t rs, int off) { CBAR(); v4u v = __builtin_amdgcn_raw_buffer_load_b128(rs, off, 0, 16); CBAR(); return v; }
DEV v2u ld8(rsrc_t rs, int off) { CBAR(); v2u v = __builtin_amdgcn_raw_buffer_load_b64(rs, off, 0, 16); CBAR(); return v; }
DEV u32 ld4(rsrc_t rs, int off) { CBAR(); u32 v = __builtin_amdgcn_raw_buffer_load_b32(rs, off, 0, 16); CBAR(); return v; }
DEV void st16(rsrc_t rs, int off, v4u v) { CBAR(); __builtin_amdgcn_raw_buffer_store_b128(v, rs, off, 0, 16); CBAR(); }
DEV void st8(rsrc_t rs, int off, v2u v) { CBAR(); __builtin_amdgcn_raw_buffer_store_b64(v, rs, off, 0, 16); CBAR(); }
DEV void st4(rsrc_t rs, int off, u32 v) { CBAR(); __builtin_amdgcn_raw_buffer_store_b32(v, rs, off, 0, 16); CBAR(); }

DEV void dma16(const void* sbase, u32 voff, u32 ldsdst) {
  u32 keep;
  asm volatile("s_mov_b32 %0, m0\n\ts_mov_b32 m0, %3\n\ts_nop 0\n\tglobal_load_lds_dwordx4 %1, %2 nt\n\ts_mov_b32 m0, %0" : "=&s"(keep) : "v"(voff), "s"(sbase), "s"(ldsdst) : "memory");
}
template <int N> DEV void vmwait() { asm volatile("s_waitcnt vmcnt(%0)" ::"i"(N) : "memory"); }
DEV void wgbar() { asm volatile("s_waitcnt lgkmcnt(0)\n\ts_barrier" ::: "memory"); }

#define TS(slot) do {} while (0)

DEV void tr4(const v2u* r, v4u* B) {
#pragma unroll
  for (int m = 0; m < 4; ++m) {
    B[0][m] = plo(r[2 * m].x, r[2 * m + 1].x); B[1][m] = phi(r[2 * m].x, r[2 * m + 1].x);
    B[2][m] = plo(r[2 * m].y, r[2 * m + 1].y); B[3][m] = phi(r[2 * m].y, r[2 * m + 1].y);
  }
  PIN(B[0]); PIN(B[1]); PIN(B[2]); PIN(B[3]);
}

__global__ void __launch_bounds__(512, 2)
fused_kernel(const u16* __restrict__ xin, const u16* __restrict__ kc, const u16* __restrict__ vc, const u16* __restrict__ wq,
             const u16* __restrict__ wo, const u16* __restrict__ wg, const u16* __restrict__ wu, const u16* __restrict__ wd,
             const u16* __restrict__ cin, const u16* __restrict__ wmod, u16* __restrict__ out, unsigned char* ws) {
  __shared__ __attribute__((aligned(16))) unsigned char lds_[L_TOTAL];
  LAS unsigned char* L = (LAS unsigned char*)lds_;
  const int tid = threadIdx.x, lane = tid & 63, wave = __builtin_amdgcn_readfirstlane(tid >> 6);
  const int bid = blockIdx.x, x = bid & 7, idx = bid >> 3, b = idx >> 3, j = idx & 7;
  const int n = lane & 15, g = lane >> 4;
  const int grp = x * 4 + b;
  const int cu = grp * 8 + j;
  const bool ismod = (j == 7);
  const rsrc_t rs = __builtin_amdgcn_make_buffer_rsrc(ws, 0, WS_END, 0x00020000);
  auto lflag = [&](int off, u32 v) { *(volatile LAS u32*)(L + off) = v; };
  auto lwait4 = [&](int off, u32 v) {
    SPIN(LSPIN) { v4u f = *(volatile LAS v4u*)(L + off); if (min(min(f.x, f.y), min(f.z, f.w)) >= v) break; __builtin_amdgcn_s_sleep(1); }
  };
  TS(0);

  if (wave < 4) {
    const int s = wave;
    const size_t kvoff = ((size_t)(b * 8 + x) << 20) + (size_t)s * 1024;
    auto co = [&](int i) { return (size_t)i * 32768 + (size_t)((j + i) & 7) * 4096; };
    const unsigned char* kp = (const unsigned char*)kc + kvoff;
    const unsigned char* vp = (const unsigned char*)vc + kvoff + lane * 16;
    const u32 lk = (u32)(uintptr_t)L + L_K + s * 1024;
    v4u va[32];
    v4u T[4][8];
    va[0] = __builtin_nontemporal_load((const v4u*)(vp + co(0)));
    SCHED();
#pragma unroll
    for (int i = 0; i < 32; ++i) {
      dma16(kp + co(i), g * 256 + ((n ^ ((i & 3) | (g << 2))) * 16), lk + i * 4096);
      if (i == WIN - 2) __builtin_amdgcn_s_barrier();
      if (i >= WIN - 1) vmwait<WIN>();
    }
    TS(1);
#pragma unroll
    for (int i = 1; i < 32; ++i) {
      va[i] = __builtin_nontemporal_load((const v4u*)(vp + co(i)));
      SCHED();
      if (i == 31) break;
      vmwait<WIN>();
      if (i == WIN) lflag(F_KDONE + 4 * s, 1);
      const int c = i - WIN;
      if (c >= 0 && (c & 1)) {
        const int G = c >> 3, m = (c & 7) >> 1;
#pragma unroll
        for (int q = 0; q < 4; ++q) { u32 t0 = plo(va[c - 1][q], va[c][q]), t1 = phi(va[c - 1][q], va[c][q]); PIN(t0); PIN(t1); T[G][2 * q][m] = t0; T[G][2 * q + 1][m] = t1; }
      }
      SCHED();
    }
    TS(2);
    lwait4(F_PDONE, 1);
    TS(3);
    v4u pa[4];
#pragma unroll
    for (int G = 0; G < 4; ++G) pa[G] = *(volatile LAS v4u*)(L + L_P + (((s * 4 + G) * 4 + g) * 4 + (n >> 2)) * 16);
    v4f acc[8];
#pragma unroll
    for (int d = 0; d < 8; ++d) acc[d] = v4f{0, 0, 0, 0};
    constexpr int CDONE = (31 - WIN) & ~1;
#pragma unroll
    for (int G = 0; G < 4; ++G) {
#pragma unroll
      for (int m = 0; m < 4; ++m) {
        const int c = G * 8 + 2 * m;
        if (c >= CDONE) {
#pragma unroll
          for (int q = 0; q < 4; ++q) { T[G][2 * q][m] = plo(va[c][q], va[c + 1][q]); T[G][2 * q + 1][m] = phi(va[c][q], va[c + 1][q]); }
          if (c < 30) SCHED();
        }
      }
#pragma unroll
      for (int d = 0; d < 8; ++d) acc[d] = mfma(pa[G], T[G][d], acc[d]);
      if (G < 3) SCHED();
    }
    *(volatile LAS v4u*)(L + L_OPW + s * 2048 + lane * 32) = v4u{canon(f2u(acc[0][0])), canon(f2u(acc[1][0])), canon(f2u(acc[2][0])), canon(f2u(acc[3][0]))};
    *(volatile LAS v4u*)(L + L_OPW + s * 2048 + lane * 32 + 16) = v4u{canon(f2u(acc[4][0])), canon(f2u(acc[5][0])), canon(f2u(acc[6][0])), canon(f2u(acc[7][0]))};
    TS(4);
    {
      v2u o0, o1, o2, o3;
      SPIN(LSPIN) {
        o0 = *(volatile LAS v2u*)(L + L_OPW + 0 * 2048 + s * 512 + lane * 8); o1 = *(volatile LAS v2u*)(L + L_OPW + 1 * 2048 + s * 512 + lane * 8);
        o2 = *(volatile LAS v2u*)(L + L_OPW + 2 * 2048 + s * 512 + lane * 8); o3 = *(volatile LAS v2u*)(L + L_OPW + 3 * 2048 + s * 512 + lane * 8);
        u32 mx = umax(umax(umax(o0.x, o0.y), umax(o1.x, o1.y)), umax(umax(o2.x, o2.y), umax(o3.x, o3.y)));
        if (!__any(mx == SENT)) break;
      }
      const float e = (u2f(o0.x) + u2f(o1.x)) + (u2f(o2.x) + u2f(o3.x)), o = (u2f(o0.y) + u2f(o1.y)) + (u2f(o2.y) + u2f(o3.y));
      st4(rs, WS_OP + cu * 1024 + s * 256 + lane * 4, canon(cvtpk(e, o)));
    }
    lflag(F_OPUB + 4 * s, 1);
    TS(5);
  } else {
    const int a = wave - 4;
    __builtin_amdgcn_s_setprio(APRIO);
    {
      int po = WS_OP + cu * 1024 + a * 256 + lane * 16;
      if (lane >= 16) po = WS_Q + grp * 1024 + a * 256 + (lane - 16) * 16;
      if (lane >= 32) po = WS_ML + cu * 128 + a * 32 + (lane - 32) * 16;
      if (lane < 34 && (ismod || g != 1)) st16(rs, po, v4u{SENT, SENT, SENT, SENT});
    }
    u32 ep = ld4(rs, WS_EPOCH);
    u32 wm[32]; v4u cv, xv; u32 xh;
    const int c0 = 128 * a + 4 * (lane & 31) + 2 * (lane >> 5);
    if (ismod) {
      cv = *(const v4u*)(cin + (n >> 2) * 512 + 128 * a + 32 * (n & 3) + 8 * g);
      xv = *(const v4u*)(xin + a * 512 + lane * 8);
      xh = *(const u32*)(xin + g * 512 + 16 * grp + 2 * (n & 7));
      const unsigned char* wp = (const unsigned char*)wmod + (size_t)(128 * a + 8 * g) * 6144 + ((n >> 3) * 512 + 16 * grp + 2 * (n & 7)) * 2;
#pragma unroll
      for (int ks = 0; ks < 4; ++ks)
#pragma unroll
        for (int i = 0; i < 8; ++i) wm[ks * 8 + i] = __builtin_nontemporal_load((const u32*)(wp + (size_t)(32 * ks + i) * 6144));
    } else {
      xh = *(const u32*)(xin + b * 512 + c0);
    }
    u32 wqr[16];
    {
      const unsigned char* wp = (const unsigned char*)wq + (size_t)(128 * a + 32 * (n >> 3) + 8 * g) * 8192 + (x * 512 + idx * 16 + 2 * (n & 7)) * 2;
#pragma unroll
      for (int ks = 0; ks < 2; ++ks)
#pragma unroll
        for (int i = 0; i < 8; ++i) wqr[ks * 8 + i] = __builtin_nontemporal_load((const u32*)(wp + (size_t)(64 * ks + i) * 8192));
    }
    SCHED();
#pragma unroll
    for (int i = 0; i < 2; ++i) *(volatile LAS v4u*)(L + L_OPW + (i * 256 + a * 64 + lane) * 16) = v4u{SENT, SENT, SENT, SENT};
    if (a == 0) *(volatile LAS u32*)(L + L_FL + lane * 4) = 0;
    wgbar();
    auto abar = [&](u32 ph) { lflag(F_ABAR + 4 * a, ph); lwait4(F_ABAR, ph); };

    v4u Bw[4][4], Bd[8], Bo[4][4];
    const int fs = 7 * x + j;
    const unsigned char* wwp; int wws, wwi0, wwi1;
    if (!ismod) { wwp = (const unsigned char*)(n < 8 ? wg : wu) + (size_t)(128 * a + 64 * (g & 1) + 2 * (g >> 1)) * 3584 + (32 * fs + 4 * (n & 7)) * 2; wws = 4 * 3584; wwi0 = 3584; wwi1 = 16 * 3584; }
    else { wwp = (const unsigned char*)wmod + (size_t)(128 * a + 8 * g) * 6144 + (1024 + 64 * grp + 4 * n) * 2; wws = 32 * 6144; wwi0 = 6144; wwi1 = 2 * 6144; }
    const unsigned char* wop = (const unsigned char*)wo + (size_t)(x * 512 + 128 * a + 8 * g) * 1024 + (64 * j + 4 * n) * 2;
    auto load_w = [&](v2u* r, int ks) {
#pragma unroll
      for (int i = 0; i < 8; ++i) r[i] = __builtin_nontemporal_load((const v2u*)(wwp + ks * wws + (i >> 1) * wwi1 + (i & 1) * wwi0));
      SCHED();
    };
    auto load_o = [&](v2u* r, int ks) {
#pragma unroll
      for (int i = 0; i < 8; ++i) r[i] = __builtin_nontemporal_load((const v2u*)(wop + (((ks + b) & 3) * 32 + i) * 1024));
      SCHED();
    };
    auto all_o = [&]() {
      v2u r0[8], r1[8];
      load_o(r0, 0); load_o(r1, 1); tr4(r0, Bo[0]); SCHED(); load_o(r0, 2); tr4(r1, Bo[1]); SCHED(); load_o(r1, 3); tr4(r0, Bo[2]); SCHED(); tr4(r1, Bo[3]); SCHED();
    };
    v4u Ac;
    u32 tag;
    if (ismod) {
      v2u r[4][8];
#pragma unroll
      for (int ks = 0; ks < 4; ++ks) load_w(r[ks], ks);
      float xs[8];
#pragma unroll
      for (int q = 0; q < 4; ++q) { xs[2 * q] = blo(xv[q]); xs[2 * q + 1] = bhi(xv[q]); }
      const float mean = wsum(((xs[0] + xs[1]) + (xs[2] + xs[3])) + ((xs[4] + xs[5]) + (xs[6] + xs[7]))) * (1.f / 512.f);
      float sq = 0;
#pragma unroll
      for (int q = 0; q < 8; ++q) sq += (xs[q] - mean) * (xs[q] - mean);
      const float var = wsum(sq) * (1.f / 512.f);
      *(volatile LAS v2u*)(L + L_XST + a * 8) = v2u{f2u(rb(mean)), f2u(rcp(rb(__builtin_amdgcn_sqrtf(rb(rb(var) + 1e-5f)))))};
#pragma unroll
      for (int q = 0; q < 4; ++q) Ac[q] = cvtpk(silu(blo(cv[q])), silu(bhi(cv[q])));
      PIN(ep);
      tag = (u32)__builtin_amdgcn_readfirstlane(ep) * 1664525u + 1013904223u;
      SCHED();
      float p0 = 0, p1 = 0;
#pragma unroll
      for (int ks = 0; ks < 4; ++ks) {
        v4u B0, B1;
#pragma unroll
        for (int m = 0; m < 4; ++m) { B0[m] = plo(wm[ks * 8 + 2 * m], wm[ks * 8 + 2 * m + 1]); B1[m] = phi(wm[ks * 8 + 2 * m], wm[ks * 8 + 2 * m + 1]); }
        p0 += mfma(Ac, B0, v4f{0, 0, 0, 0})[ks]; p1 += mfma(Ac, B1, v4f{0, 0, 0, 0})[ks];
      }
      *(volatile LAS v2f*)(L + L_MP + lane * 32 + a * 8) = v2f{p0, p1};
      abar(1);
      {
        const int o = L_MP + (16 * g + (n & 7)) * 32;
        const v4f s0 = *(volatile LAS v4f*)(L + o), s1 = *(volatile LAS v4f*)(L + o + 16), c0_ = *(volatile LAS v4f*)(L + o + 256), c1_ = *(volatile LAS v4f*)(L + o + 272);
        const v2f st = *(volatile LAS v2f*)(L + L_XST + g * 8);
        const u32 sa = cvtpk((s0[0] + s0[2]) + (s1[0] + s1[2]), (s0[1] + s0[3]) + (s1[1] + s1[3]));
        const u32 ca = cvtpk((c0_[0] + c0_[2]) + (c1_[0] + c1_[2]), (c0_[1] + c0_[3]) + (c1_[1] + c1_[3]));
        const u32 c1 = cvtpk(1.f + blo(ca), 1.f + bhi(ca));
        const u32 d = cvtpk(blo(xh) - st[0], bhi(xh) - st[0]);
        const u32 nn = cvtpk(blo(d) * st[1], bhi(d) * st[1]);
        const u32 t = cvtpk(blo(nn) * blo(c1), bhi(nn) * bhi(c1));
        const u32 h = cvtpk(blo(t) + blo(sa), bhi(t) + bhi(sa));
        st8(rs, WS_H + (2 * a + (n >> 3)) * 8192 + grp * 256 + g * 64 + (n & 7) * 8, v2u{h, tag});
      }
      TS(1);
#pragma unroll
      for (int ks = 0; ks < 4; ++ks) tr4(r[ks], Bw[ks]);
#pragma unroll
      for (int q = 0; q < 8; ++q) Bd[q] = v4u{0, 0, 0, 0};
      if (WO_EARLY) all_o();
    } else {
      v2u r0[8], r1[8]; v4u rd[8];
      const unsigned char* dp = (const unsigned char*)wd + (size_t)(32 * fs + 8 * g) * 1024 + (128 * a + 8 * n) * 2;
      load_w(r0, 0); load_w(r1, 1); tr4(r0, Bw[0]); SCHED(); load_w(r0, 2); tr4(r1, Bw[1]); SCHED(); load_w(r1, 3); tr4(r0, Bw[2]); SCHED();
#pragma unroll
      for (int i = 0; i < 8; ++i) rd[i] = __builtin_nontemporal_load((const v4u*)(dp + i * 1024));
      SCHED();
      tr4(r1, Bw[3]); SCHED();
#pragma unroll
      for (int m = 0; m < 4; ++m)
#pragma unroll
        for (int q = 0; q < 4; ++q) { Bd[2 * q][m] = plo(rd[2 * m][q], rd[2 * m + 1][q]); Bd[2 * q + 1][m] = phi(rd[2 * m][q], rd[2 * m + 1][q]); }
#pragma unroll
      for (int q = 0; q < 8; ++q) PIN(Bd[q]);
      if (WO_EARLY) all_o();
      PIN(ep);
      tag = (u32)__builtin_amdgcn_readfirstlane(ep) * 1664525u + 1013904223u;
      Ac = v4u{0, 0, 0, 0};
      TS(1);
    }
    {
      v4u B[2][2];
#pragma unroll
      for (int ks = 0; ks < 2; ++ks)
#pragma unroll
        for (int m = 0; m < 4; ++m) { B[ks][0][m] = plo(wqr[ks * 8 + 2 * m], wqr[ks * 8 + 2 * m + 1]); B[ks][1][m] = phi(wqr[ks * 8 + 2 * m], wqr[ks * 8 + 2 * m + 1]); }
      PIN(B[0][0]); PIN(B[0][1]); PIN(B[1][0]); PIN(B[1][1]);
      const int k0 = 128 * a + 32 * (n & 3) + 8 * g;
      const int hoff = WS_H + x * 8192 + (k0 >> 4) * 256 + (n >> 2) * 64 + (k0 & 15) * 4;
      v4u h0, h1;
      SPIN(GSPIN) {
        h0 = ld16(rs, hoff); h1 = ld16(rs, hoff + 16);
        if (!__any(((h0.y ^ tag) | (h0.w ^ tag) | (h1.y ^ tag) | (h1.w ^ tag)) != 0)) break;
        __builtin_amdgcn_s_sleep(D1);
      }
      TS(2);
      const v4u A = {h0.x, h0.z, h1.x, h1.z};
      float pt[2];
#pragma unroll
      for (int cc = 0; cc < 2; ++cc) {
        const v4f d0 = mfma(A, B[0][cc], v4f{0, 0, 0, 0}), d1 = mfma(A, B[1][cc], v4f{0, 0, 0, 0});
        const float lo = d0[0] + d1[2], hi = d0[1] + d1[3], p = n < 8 ? lo : hi;
        pt[cc] = p + dppf<ROR8>(p);
      }
      *(volatile LAS v2f*)(L + L_QP + (g * 8 + (n & 7)) * 32 + a * 8) = v2f{pt[0], pt[1]};
      abar(2);
      if (a == 0) {
        const v4f s0 = *(volatile LAS v4f*)(L + L_QP + (lane & 31) * 32), s1 = *(volatile LAS v4f*)(L + L_QP + (lane & 31) * 32 + 16);
        const u32 qp = canon(cvtpk((s0[0] + s0[2]) + (s1[0] + s1[2]), (s0[1] + s0[3]) + (s1[1] + s1[3])));
        if (lane < 32) st4(rs, WS_Q + (x * 32 + idx) * 128 + lane * 4, qp);
      }
    }
    TS(3);
    {
      v4u qa[4];
      const int qoff = WS_Q + (x * 32 + (n & 3) * 8 + (g >> 1)) * 128 + b * 32 + (g & 1) * 16;
      __builtin_amdgcn_s_sleep(D2);
      SPIN(GSPIN) {
#pragma unroll
        for (int ks = 0; ks < 4; ++ks) qa[ks] = ld16(rs, qoff + ks * 256);
        if (!__any(umax(umax(max4(qa[0]), max4(qa[1])), umax(max4(qa[2]), max4(qa[3]))) == SENT)) break;
        __builtin_amdgcn_s_sleep(D1);
      }
      TS(4);
      lwait4(F_KDONE, 1);
      TS(5);
      float sc[8];
#pragma unroll
      for (int u = 0; u < 2; ++u) {
        v4f D[4];
#pragma unroll
        for (int t = 0; t < 4; ++t) {
          D[t] = v4f{0, 0, 0, 0};
#pragma unroll
          for (int ks = 0; ks < 4; ++ks) {
            const v4u kf = *(const LAS v4u*)(L + L_K + (128 * a + 64 * u + 16 * (n & 3) + 4 * t + (n >> 2)) * 256 + (((4 * ks + g) ^ n) * 16));
            D[t] = mfma(kf, qa[ks], D[t]);
          }
        }
#pragma unroll
        for (int r = 0; r < 4; r += 2) {
          const u32 w = cvtpk(sel4(n >> 2, D[0][r], D[1][r], D[2][r], D[3][r]), sel4(n >> 2, D[0][r + 1], D[1][r + 1], D[2][r + 1], D[3][r + 1]));
          const u32 w2 = cvtpk(blo(w) * 0.08838834764831845f, bhi(w) * 0.08838834764831845f);
          sc[4 * u + r] = blo(w2); sc[4 * u + r + 1] = bhi(w2);
        }
      }
      float m = fmaxf(fmaxf(fmaxf(sc[0], sc[1]), fmaxf(sc[2], sc[3])), fmaxf(fmaxf(sc[4], sc[5]), fmaxf(sc[6], sc[7])));
      m = fmaxf(m, dppf<ROR4>(m)); m = fmaxf(m, dppf<ROR8>(m)); m = max32(max16(m));
      *(volatile LAS float*)(L + L_MX + (n & 3) * 16 + a * 4) = m;
      abar(3);
      { const v4f t = *(volatile LAS v4f*)(L + L_MX + (n & 3) * 16); m = fmaxf(fmaxf(t[0], t[1]), fmaxf(t[2], t[3])); }
      float e[8];
#pragma unroll
      for (int q = 0; q < 8; ++q) e[q] = ex2((sc[q] - m) * LOG2E);
      *(volatile LAS v4u*)(L + L_P + ((((n >> 2) * 4 + a) * 4 + g) * 4 + (n & 3)) * 16) = v4u{cvtpk(e[0], e[1]), cvtpk(e[2], e[3]), cvtpk(e[4], e[5]), cvtpk(e[6], e[7])};
      lflag(F_PDONE + 4 * a, 1);
      float l = ((e[0] + e[1]) + (e[2] + e[3])) + ((e[4] + e[5]) + (e[6] + e[7]));
      l += dppf<ROR4>(l); l += dppf<ROR8>(l); l = sum32(sum16(l));
      if (lane < 4) st8(rs, WS_ML + cu * 128 + lane * 32 + a * 8, v2u{canon(f2u(m)), canon(f2u(l))});
    }
    TS(6);
    if (!WO_EARLY) all_o();
    TS(7);
    float Cc = 0, Bc = 0, xc0 = 0, xc1 = 0, gc0 = 0, gc1 = 0, ga0 = 0, ga1 = 0; u32 gbp = 0;
    const int ocol = 128 * (x >> 1) + 64 * j + 8 * (lane & 7) + 4 * (x & 1) + g;
    if (ismod) {
      float pt[4];
#pragma unroll
      for (int cc = 0; cc < 4; ++cc) {
        pt[cc] = 0;
#pragma unroll
        for (int ks = 0; ks < 4; ++ks) pt[cc] += mfma(Ac, Bw[ks][cc], v4f{0, 0, 0, 0})[ks];
        *(volatile LAS float*)(L + L_LP + (g * 64 + 4 * n + cc) * 16 + a * 4) = pt[cc];
      }
      abar(4);
      const v4f t0 = *(volatile LAS v4f*)(L + L_LP + (a * 64 + 2 * (lane & 31)) * 16), t1 = *(volatile LAS v4f*)(L + L_LP + (a * 64 + 2 * (lane & 31)) * 16 + 16);
      const v2u w = {cvtpk(sum4(t0), sum4(t1)), tag};
#pragma unroll
      for (int i = 0; i < 4; ++i) st8(rs, WS_MODL + ((2 * i + (lane >> 5)) * 4 + a) * 8192 + grp * 256 + (lane & 31) * 8, w);
    } else {
      v2u ga, sb, cb, gb;
      const int moff = WS_MODL + grp * 8192;
      SPIN(GSPIN) {
        ga = ld8(rs, moff + c0 * 4); sb = ld8(rs, moff + 2048 + c0 * 4); cb = ld8(rs, moff + 4096 + c0 * 4); gb = ld8(rs, moff + 6144 + (((ocol & 511) >> 1) * 8));
        if (!__any(((ga.y ^ tag) | (sb.y ^ tag) | (cb.y ^ tag) | (gb.y ^ tag)) != 0)) break;
        __builtin_amdgcn_s_sleep(8);
      }
      const u32 c1 = cvtpk(1.f + blo(cb.x), 1.f + bhi(cb.x));
      ga0 = blo(ga.x); ga1 = bhi(ga.x); gbp = (ocol & 1) ? (gb.x & 0xFFFF0000u) : (gb.x << 16);
      xc0 = blo(xh) * blo(c1); xc1 = bhi(xh) * bhi(c1); gc0 = ga0 * blo(c1); gc1 = ga1 * bhi(c1);
      *(volatile LAS u32*)(L + L_AP + a * 512 + (g * 4 + (n & 3)) * 16 + (n >> 2) * 4) = c1;
      *(volatile LAS u32*)(L + L_AP + a * 512 + 256 + (g * 4 + (n & 3)) * 16 + (n >> 2) * 4) = sb.x;
      const v4u A = *(volatile LAS v4u*)(L + L_AP + a * 512 + ((n >> 2) & 1) * 256 + (g * 4 + (n & 3)) * 16);
#pragma unroll
      for (int cc = 0; cc < 4; ++cc) {
        float p = 0;
#pragma unroll
        for (int ks = 0; ks < 4; ++ks) p += mfma(A, Bw[ks][cc], v4f{0, 0, 0, 0})[ks];
        *(volatile LAS float*)(L + L_LP + (g * 64 + 4 * n + cc) * 16 + a * 4) = p;
      }
      abar(4);
      const int f = (n >> 3) * 32 + 8 * g + (n & 7);
      Cc = sum4(*(volatile LAS v4f*)(L + L_LP + f * 16)); Bc = sum4(*(volatile LAS v4f*)(L + L_LP + 1024 + f * 16));
    }
    TS(8);
    float fr[4];
    {
      v2u t;
      SPIN(GSPIN) {
        t = ld8(rs, WS_ML + (grp * 8 + ((lane >> 2) & 7)) * 128 + a * 32 + (lane & 3) * 8);
        if (!__any(umax(t.x, t.y) == SENT)) break;
        __builtin_amdgcn_s_sleep(4);
      }
      const float m = u2f(t.x);
      float M = fmaxf(m, dppf<ROR4>(m)); M = fmaxf(M, dppf<ROR8>(M)); M = max16(M);
      const float w = ex2((m - M) * LOG2E);
      float l = u2f(t.y) * w;
      l += dppf<XOR1>(l); l += dppf<XOR2>(l); l += dppf<ROR4>(l); l += dppf<ROR8>(l); l = sum16(l);
      const float f = w / l;
      fr[0] = dppf<NBC + 0>(f); fr[1] = dppf<NBC + 4>(f); fr[2] = dppf<NBC + 8>(f); fr[3] = dppf<NBC + 12>(f);
      PIN(fr[0]); PIN(fr[1]); PIN(fr[2]); PIN(fr[3]);
      PIN(Cc); PIN(Bc); PIN(xc0); PIN(xc1); PIN(gc0); PIN(gc1); PIN(ga0); PIN(ga1); PIN(gbp);
    }
    TS(9);
    {
      lwait4(F_OPUB, 1);
      __builtin_amdgcn_s_sleep(D3);
      v4u oa[4];
      const int ooff = WS_OP + (grp * 8 + (n & 7)) * 1024 + (128 * a + 8 * g) * 2;
      SPIN(GSPIN) {
#pragma unroll
        for (int ks = 0; ks < 4; ++ks) oa[ks] = ld16(rs, ooff + ((ks + b) & 3) * 64);
        if (!__any(umax(umax(max4(oa[0]), max4(oa[1])), umax(max4(oa[2]), max4(oa[3]))) == SENT)) break;
      }
      TS(10);
      float t[4];
#pragma unroll
      for (int cc = 0; cc < 4; ++cc) {
        v4f ac = v4f{0, 0, 0, 0};
#pragma unroll
        for (int ks = 0; ks < 4; ++ks) ac = mfma(oa[ks], Bo[ks][cc], ac);
        t[cc] = (ac[0] * fr[0] + ac[1] * fr[1]) + (ac[2] * fr[2] + ac[3] * fr[3]);
      }
      swap16(t[0], t[1]); swap16(t[2], t[3]);
      const float y01 = t[0] + t[1], y23 = t[2] + t[3];
      *(volatile LAS float*)(L + L_YP + (4 * n + g) * 16 + a * 4) = lane < 32 ? y01 : y23;
      wgbar();
      const v4f y0 = *(volatile LAS v4f*)(L + L_YP + (lane & 31) * 32), y1 = *(volatile LAS v4f*)(L + L_YP + (lane & 31) * 32 + 16);
      st8(rs, WS_Y + (((2 * a + (lane >> 5)) * 4 + b) * 8 + x) * 2048 + j * 256 + (lane & 31) * 8, v2u{cvtpk(sum4(y0), sum4(y1)), tag});
    }
    TS(11);
    if (!ismod) {
      __builtin_amdgcn_s_sleep(D4);
      v4u y[4];
      const int yoff = WS_Y + (grp * 8 + (lane >> 5)) * 2048 + (128 * a + 4 * (lane & 31)) * 4;
      SPIN(GSPIN) {
#pragma unroll
        for (int i = 0; i < 4; ++i) y[i] = ld16(rs, yoff + i * 4096);
        const u32 bad = ((y[0].y ^ tag) | (y[0].w ^ tag)) | ((y[1].y ^ tag) | (y[1].w ^ tag)) | ((y[2].y ^ tag) | (y[2].w ^ tag)) | ((y[3].y ^ tag) | (y[3].w ^ tag));
        if (!__any(bad != 0)) break;
      }
      TS(12);
      float v0 = (blo(y[0].x) + blo(y[1].x)) + (blo(y[2].x) + blo(y[3].x)), v1 = (bhi(y[0].x) + bhi(y[1].x)) + (bhi(y[2].x) + bhi(y[3].x));
      float v2 = (blo(y[0].z) + blo(y[1].z)) + (blo(y[2].z) + blo(y[3].z)), v3 = (bhi(y[0].z) + bhi(y[1].z)) + (bhi(y[2].z) + bhi(y[3].z));
      swap32(v0, v2); swap32(v1, v3);
      const float ya = v0 + v2, yb = v1 + v3;
      const u32 hp = cvtpk(ya * gc0 + xc0, yb * gc1 + xc1);
      const v4u A = {hp, dppu<SHL + 4>(hp), dppu<SHL + 8>(hp), dppu<SHL + 12>(hp)};
      const float xa = ya * ga0 + blo(xh), xb = yb * ga1 + bhi(xh);
      float pt[4];
#pragma unroll
      for (int cc = 0; cc < 4; ++cc) {
        v4f d[4];
#pragma unroll
        for (int ks = 0; ks < 4; ++ks) d[ks] = mfma(A, Bw[ks][cc], v4f{0, 0, 0, 0});
        pt[cc] = (d[0][0] + d[1][1]) + (d[2][2] + d[3][3]);
      }
      const float s1 = wsum(xa + xb), s2 = wsum(xa * xa + xb * xb);
      *(volatile LAS v2f*)(L + L_ST + a * 8) = v2f{s1, s2};
      if (j < 2) *(volatile LAS v2f*)(L + L_X1 + c0 * 4) = v2f{xa, xb};
      if (g == 0) {
#pragma unroll
        for (int cc = 0; cc < 4; ++cc) *(volatile LAS float*)(L + L_GP + (4 * n + cc) * 16 + a * 4) = pt[cc];
      }
      wgbar();
      const v4f t = *(volatile LAS v4f*)(L + L_GP + ((n >> 3) * 32 + 8 * g + (n & 7)) * 16);
      const v4f q0 = *(volatile LAS v4f*)(L + L_ST), q1 = *(volatile LAS v4f*)(L + L_ST + 16);
      const float mu = ((q0[0] + q0[2]) + (q1[0] + q1[2])) * (1.f / 512.f), e2 = ((q0[1] + q0[3]) + (q1[1] + q1[3])) * (1.f / 512.f);
      const float rstd = __builtin_amdgcn_rsqf(fmaxf(e2 - mu * mu, 0.f) + 1e-5f);
      const float val = (sum4(t) - mu * Cc) * rstd + Bc;
      const float act = silu(val) * dppf<ROR8>(val);
      const u32 ap = cvtpk(act, dppf<SHL + 1>(act));
      const v4u A2 = {ap, dppu<SHL + 2>(ap), dppu<SHL + 4>(ap), dppu<SHL + 6>(ap)};
      float z[8];
#pragma unroll
      for (int cc = 0; cc < 8; ++cc) z[cc] = mfma(A2, Bd[cc], v4f{0, 0, 0, 0})[0];
      if (g == 0) {
        const int zo = WS_Z + (b * 56 + fs) * 2048 + a * 512 + n * 16;
        st16(rs, zo, v4u{cvtpk(z[0], z[1]), tag, cvtpk(z[2], z[3]), tag});
        st16(rs, zo + 256, v4u{cvtpk(z[4], z[5]), tag, cvtpk(z[6], z[7]), tag});
      }
      TS(13);
      if (a == 0 && j < 2) {
        const float x1 = *(volatile LAS float*)(L + L_X1 + ocol * 4);
        if (bid == 0 && lane == 0) st4(rs, WS_EPOCH, tag);
        __builtin_amdgcn_s_sleep(D5);
        v4u zz[7];
        const int zoff = WS_Z + (b * 56 + (lane >> 3)) * 2048 + (2 * x + j) * 128 + (lane & 7) * 16;
        SPIN(GSPIN) {
          u32 bad = 0;
#pragma unroll
          for (int i = 0; i < 7; ++i) zz[i] = ld16(rs, zoff + i * 16384);
#pragma unroll
          for (int i = 0; i < 7; ++i) bad |= (zz[i].y ^ tag) | (zz[i].w ^ tag);
          if (!__any(bad != 0)) break;
        }
        TS(14);
        float w0 = 0, w1 = 0, w2 = 0, w3 = 0;
#pragma unroll
        for (int i = 0; i < 7; ++i) { w0 += blo(zz[i].x); w1 += bhi(zz[i].x); w2 += blo(zz[i].z); w3 += bhi(zz[i].z); }
        swap32(w0, w2); swap32(w1, w3);
        float r0 = w0 + w2, r1 = w1 + w3;
        swap16(r0, r1);
        float r = r0 + r1;
        r += dppf<ROR8>(r);
        if (n < 8) out[b * 512 + ocol] = (u16)cvtpk(x1 + u2f(gbp) * r, 0.f);
      }
    }
  }
  TS(15);
}

extern "C" void kernel_launch(void* const* d_in, const int* in_sizes, int n_in, void* d_out, int out_size, void* d_ws, size_t ws_size,
                              hipStream_t stream) {
  fused_kernel<<<256, 512, 0, stream>>>((const u16*)d_in[0], (const u16*)d_in[1], (const u16*)d_in[2], (const u16*)d_in[3], (const u16*)d_in[4],
                                        (const u16*)d_in[6], (const u16*)d_in[7], (const u16*)d_in[8], (const u16*)d_in[9], (const u16*)d_in[10],
                                        (u16*)d_out, (unsigned char*)d_ws);
}
```

```cpp
#include <hip/hip_runtime.h>
#include <hip/hip_bf16.h>
#include <stdint.h>

typedef unsigned u32;
typedef unsigned short u16;
typedef unsigned long long u64;
typedef u32 v4u __attribute__((ext_vector_type(4)));
typedef u32 v2u __attribute__((ext_vector_type(2)));
typedef float v4f __attribute__((ext_vector_type(4)));
typedef float v2f __attribute__((ext_vector_type(2)));
typedef __bf16 v2b __attribute__((ext_vector_type(2)));
typedef short v8s __attribute__((ext_vector_type(8)));
#define LAS __attribute__((address_space(3)))
#define DEV __device__ __forceinline__

#define WIN 8
#define AW 8
#define D1 4
#define D2 8
#define D3 10
#define D4 10
#define D5 10
#define APRIO 3
#define WO_EARLY 0

constexpr int WS_EPOCH = 0;
constexpr int WS_H = 4096;
constexpr int WS_Q = WS_H + 65536;
constexpr int WS_ML = WS_Q + 32768;
constexpr int WS_OP = WS_ML + 32768;
constexpr int WS_MODL = WS_OP + 262144;
constexpr int WS_Y = WS_MODL + 262144;
constexpr int WS_Z = WS_Y + 524288;
constexpr int WS_END = WS_Z + 4 * 56 * 2048;
constexpr int WS_STAMP = 4 << 20;

constexpr int L_K = 0;
constexpr int L_P = 131072;
constexpr int L_OPW = L_P + 4096;
constexpr int L_FL = L_OPW + 8192;
constexpr int F_KDONE = L_FL, F_PDONE = L_FL + 16, F_OPUB = L_FL + 32, F_ABAR = L_FL + 48;
constexpr int L_MP = L_FL + 256;
constexpr int L_XST = L_MP + 2048;
constexpr int L_QP = L_XST + 64;
constexpr int L_MX = L_QP + 1024;
constexpr int L_LP = L_MX + 64;
constexpr int L_AP = L_LP + 4096;
constexpr int L_YP = L_AP + 2048;
constexpr int L_GP = L_YP + 1024;
constexpr int L_ST = L_GP + 1024;
constexpr int L_X1 = L_ST + 64;
constexpr int L_TOTAL = L_X1 + 2048;

constexpr u32 SENT = 0xFFFFFFFFu;
constexpr int GSPIN = 1 << 16, LSPIN = 1 << 20;

DEV float u2f(u32 u) { return __builtin_bit_cast(float, u); }
DEV u32 f2u(float f) { return __builtin_bit_cast(u32, f); }
DEV u32 cvtpk(float a, float b) { v2f f = {a, b}; return __builtin_bit_cast(u32, __builtin_convertvector(f, v2b)); }
DEV float blo(u32 p) { return u2f(p << 16); }
DEV float bhi(u32 p) { return u2f(p & 0xFFFF0000u); }
DEV float rb(float f) { return blo(cvtpk(f, f)); }
DEV u32 canon(u32 u) { return u < 0xFFFFFFFEu ? u : 0xFFFFFFFEu; }
DEV float ex2(float v) { return __builtin_amdgcn_exp2f(v); }
DEV float rcp(float v) { return __builtin_amdgcn_rcpf(v); }
constexpr float LOG2E = 1.4426950408889634f;
DEV float silu(float v) { return v * rcp(1.f + ex2(-LOG2E * v)); }
template <int C> DEV u32 dppu(u32 v) { return (u32)__builtin_amdgcn_mov_dpp((int)v, C, 0xf, 0xf, true); }
template <int C> DEV float dppf(float v) { return u2f(dppu<C>(f2u(v))); }
constexpr int XOR1 = 0xB1, XOR2 = 0x4E, ROR4 = 0x124, ROR8 = 0x128, SHL = 0x100, NBC = 0x150;
DEV void swap32(float& a, float& b) { auto r = __builtin_amdgcn_permlane32_swap(f2u(a), f2u(b), false, false); a = u2f(r[0]); b = u2f(r[1]); }
DEV void swap16(float& a, float& b) { auto r = __builtin_amdgcn_permlane16_swap(f2u(a), f2u(b), false, false); a = u2f(r[0]); b = u2f(r[1]); }
DEV float sum16(float v) { float a = v, b = v; swap16(a, b); return a + b; }
DEV float sum32(float v) { float a = v, b = v; swap32(a, b); return a + b; }
DEV float max16(float v) { float a = v, b = v; swap16(a, b); return fmaxf(a, b); }
DEV float max32(float v) { float a = v, b = v; swap32(a, b); return fmaxf(a, b); }
DEV float wsum(float v) { v += dppf<XOR1>(v); v += dppf<XOR2>(v); v += dppf<ROR4>(v); v += dppf<ROR8>(v); return sum32(sum16(v)); }
DEV v4f mfma(v4u a, v4u b, v4f c) { return __builtin_amdgcn_mfma_f32_16x16x32_bf16(__builtin_bit_cast(v8s, a), __builtin_bit_cast(v8s, b), c, 0, 0, 0); }
DEV u32 plo(u32 k0, u32 k1) { return __builtin_amdgcn_perm(k1, k0, 0x05040100u); }
DEV u32 phi(u32 k0, u32 k1) { return __builtin_amdgcn_perm(k1, k0, 0x07060302u); }
DEV float sel4(int g, float a, float b, float c, float d) { float lo = (g & 1) ? b : a, hi = (g & 1) ? d : c; return (g & 2) ? hi : lo; }
DEV u32 umax(u32 a, u32 b) { return a > b ? a : b; }
DEV u32 max4(v4u v) { return umax(umax(v.x, v.y), umax(v.z, v.w)); }
DEV float sum4(v4f v) { return (v[0] + v[1]) + (v[2] + v[3]); }
#define CBAR() asm volatile("" ::: "memory")
#define SCHED() __builtin_amdgcn_sched_barrier(0)
#define PIN(v) asm volatile("" : "+v"(v))
#define SPIN(N) _Pragma("unroll 1") for (int sp = 0; sp < (N); ++sp)

typedef __amdgpu_buffer_rsrc_t rsrc_t;
DEV v4u ld16(rsrc_t rs, int off) { CBAR(); v4u v = __builtin_amdgcn_raw_buffer_load_b128(rs, off, 0, 16); CBAR(); return v; }
DEV v2u ld8(rsrc_t rs, int off) { CBAR(); v2u v = __builtin_amdgcn_raw_buffer_load_b64(rs, off, 0, 16); CBAR(); return v; }
DEV u32 ld4(rsrc_t rs, int off) { CBAR(); u32 v = __builtin_amdgcn_raw_buffer_load_b32(rs, off, 0, 16); CBAR(); return v; }
DEV void st16(rsrc_t rs, int off, v4u v) { CBAR(); __builtin_amdgcn_raw_buffer_store_b128(v, rs, off, 0, 16); CBAR(); }
DEV void st8(rsrc_t rs, int off, v2u v) { CBAR(); __builtin_amdgcn_raw_buffer_store_b64(v, rs, off, 0, 16); CBAR(); }
DEV void st4(rsrc_t rs, int off, u32 v) { CBAR(); __builtin_amdgcn_raw_buffer_store_b32(v, rs, off, 0, 16); CBAR(); }

DEV void dma16(const void* sbase, u32 voff, u32 ldsdst) {
  u32 keep;
  asm volatile("s_mov_b32 %0, m0\n\ts_mov_b32 m0, %3\n\ts_nop 0\n\tglobal_load_lds_dwordx4 %1, %2 nt\n\ts_mov_b32 m0, %0" : "=&s"(keep) : "v"(voff), "s"(sbase), "s"(ldsdst) : "memory");
}
template <int N> DEV void vmwait() { asm volatile("s_waitcnt vmcnt(%0)" ::"i"(N) : "memory"); }
DEV void wgbar() { asm volatile("s_waitcnt lgkmcnt(0)\n\ts_barrier" ::: "memory"); }

#define TS(slot) do {} while (0)

DEV void tr4(const v2u* r, v4u* B) {
#pragma unroll
  for (int m = 0; m < 4; ++m) {
    B[0][m] = plo(r[2 * m].x, r[2 * m + 1].x); B[1][m] = phi(r[2 * m].x, r[2 * m + 1].x);
    B[2][m] = plo(r[2 * m].y, r[2 * m + 1].y); B[3][m] = phi(r[2 * m].y, r[2 * m + 1].y);
  }
  PIN(B[0]); PIN(B[1]); PIN(B[2]); PIN(B[3]);
}

__global__ void __launch_bounds__(512, 2)
fused_kernel(const u16* __restrict__ xin, const u16* __restrict__ kc, const u16* __restrict__ vc, const u16* __restrict__ wq,
             const u16* __restrict__ wo, const u16* __restrict__ wg, const u16* __restrict__ wu, const u16* __restrict__ wd,
             const u16* __restrict__ cin, const u16* __restrict__ wmod, u16* __restrict__ out, unsigned char* ws) {
  __shared__ __attribute__((aligned(16))) unsigned char lds_[L_TOTAL];
  LAS unsigned char* L = (LAS unsigned char*)lds_;
  const int tid = threadIdx.x, lane = tid & 63, wave = __builtin_amdgcn_readfirstlane(tid >> 6);
  const int bid = blockIdx.x, x = bid & 7, idx = bid >> 3, b = idx >> 3, j = idx & 7;
  const int n = lane & 15, g = lane >> 4;
  const int grp = x * 4 + b;
  const int cu = grp * 8 + j;
  const bool ismod = (j == 7);
  const rsrc_t rs = __builtin_amdgcn_make_buffer_rsrc(ws, 0, WS_END, 0x00020000);
  auto lflag = [&](int off, u32 v) { *(volatile LAS u32*)(L + off) = v; };
  auto lwait4 = [&](int off, u32 v) {
    SPIN(LSPIN) { v4u f = *(volatile LAS v4u*)(L + off); if (min(min(f.x, f.y), min(f.z, f.w)) >= v) break; __builtin_amdgcn_s_sleep(1); }
  };
  TS(0);
  if (bid == 0 && tid == 256) *(volatile u64*)(ws + (8 << 20)) = __builtin_amdgcn_s_memrealtime();

  if (wave < 4) {
    const int s = wave;
    const size_t kvoff = ((size_t)(b * 8 + x) << 20) + (size_t)s * 1024;
    auto co = [&](int i) { return (size_t)i * 32768 + (size_t)((j + i) & 7) * 4096; };
    const unsigned char* kp = (const unsigned char*)kc + kvoff;
    const unsigned char* vp = (const unsigned char*)vc + kvoff + lane * 16;
    const u32 lk = (u32)(uintptr_t)L + L_K + s * 1024;
    v4u va[32];
    v4u T[4][8];
    va[0] = __builtin_nontemporal_load((const v4u*)(vp + co(0)));
    SCHED();
#pragma unroll
    for (int i = 0; i < 32; ++i) {
      dma16(kp + co(i), g * 256 + ((n ^ ((i & 3) | (g << 2))) * 16), lk + i * 4096);
      if (i == WIN - 2) __builtin_amdgcn_s_barrier();
      if (i >= WIN - 1) vmwait<WIN>();
    }
    TS(1);
#pragma unroll
    for (int i = 1; i < 32; ++i) {
      va[i] = __builtin_nontemporal_load((const v4u*)(vp + co(i)));
      SCHED();
      if (i == 31) break;
      vmwait<WIN>();
      if (i == WIN) lflag(F_KDONE + 4 * s, 1);
      const int c = i - WIN;
      if (c >= 0 && (c & 1)) {
        const int G = c >> 3, m = (c & 7) >> 1;
#pragma unroll
        for (int q = 0; q < 4; ++q) { u32 t0 = plo(va[c - 1][q], va[c][q]), t1 = phi(va[c - 1][q], va[c][q]); PIN(t0); PIN(t1); T[G][2 * q][m] = t0; T[G][2 * q + 1][m] = t1; }
      }
      SCHED();
    }
    TS(2);
    lwait4(F_PDONE, 1);
    TS(3);
    v4u pa[4];
#pragma unroll
    for (int G = 0; G < 4; ++G) pa[G] = *(volatile LAS v4u*)(L + L_P + (((s * 4 + G) * 4 + g) * 4 + (n >> 2)) * 16);
    v4f acc[8];
#pragma unroll
    for (int d = 0; d < 8; ++d) acc[d] = v4f{0, 0, 0, 0};
    constexpr int CDONE = (31 - WIN) & ~1;
#pragma unroll
    for (int G = 0; G < 4; ++G) {
#pragma unroll
      for (int m = 0; m < 4; ++m) {
        const int c = G * 8 + 2 * m;
        if (c >= CDONE) {
#pragma unroll
          for (int q = 0; q < 4; ++q) { T[G][2 * q][m] = plo(va[c][q], va[c + 1][q]); T[G][2 * q + 1][m] = phi(va[c][q], va[c + 1][q]); }
          if (c < 30) SCHED();
        }
      }
#pragma unroll
      for (int d = 0; d < 8; ++d) acc[d] = mfma(pa[G], T[G][d], acc[d]);
      if (G < 3) SCHED();
    }
    *(volatile LAS v4u*)(L + L_OPW + s * 2048 + lane * 32) = v4u{canon(f2u(acc[0][0])), canon(f2u(acc[1][0])), canon(f2u(acc[2][0])), canon(f2u(acc[3][0]))};
    *(volatile LAS v4u*)(L + L_OPW + s * 2048 + lane * 32 + 16) = v4u{canon(f2u(acc[4][0])), canon(f2u(acc[5][0])), canon(f2u(acc[6][0])), canon(f2u(acc[7][0]))};
    TS(4);
    {
      v2u o0, o1, o2, o3;
      SPIN(LSPIN) {
        o0 = *(volatile LAS v2u*)(L + L_OPW + 0 * 2048 + s * 512 + lane * 8); o1 = *(volatile LAS v2u*)(L + L_OPW + 1 * 2048 + s * 512 + lane * 8);
        o2 = *(volatile LAS v2u*)(L + L_OPW + 2 * 2048 + s * 512 + lane * 8); o3 = *(volatile LAS v2u*)(L + L_OPW + 3 * 2048 + s * 512 + lane * 8);
        u32 mx = umax(umax(umax(o0.x, o0.y), umax(o1.x, o1.y)), umax(umax(o2.x, o2.y), umax(o3.x, o3.y)));
        if (!__any(mx == SENT)) break;
      }
      const float e = (u2f(o0.x) + u2f(o1.x)) + (u2f(o2.x) + u2f(o3.x)), o = (u2f(o0.y) + u2f(o1.y)) + (u2f(o2.y) + u2f(o3.y));
      st4(rs, WS_OP + cu * 1024 + s * 256 + lane * 4, canon(cvtpk(e, o)));
    }
    lflag(F_OPUB + 4 * s, 1);
    TS(5);
  } else {
    const int a = wave - 4;
    __builtin_amdgcn_s_setprio(APRIO);
    {
      int po = WS_OP + cu * 1024 + a * 256 + lane * 16;
      if (lane >= 16) po = WS_Q + grp * 1024 + a * 256 + (lane - 16) * 16;
      if (lane >= 32) po = WS_ML + cu * 128 + a * 32 + (lane - 32) * 16;
      if (lane < 34 && (ismod || g != 1)) st16(rs, po, v4u{SENT, SENT, SENT, SENT});
    }
    u32 ep = ld4(rs, WS_EPOCH);
    u32 wm[32]; v4u cv, xv; u32 xh;
    const int c0 = 128 * a + 4 * (lane & 31) + 2 * (lane >> 5);
    if (ismod) {
      cv = *(const v4u*)(cin + (n >> 2) * 512 + 128 * a + 32 * (n & 3) + 8 * g);
      xv = *(const v4u*)(xin + a * 512 + lane * 8);
      xh = *(const u32*)(xin + g * 512 + 16 * grp + 2 * (n & 7));
      const unsigned char* wp = (const unsigned char*)wmod + (size_t)(128 * a + 8 * g) * 6144 + ((n >> 3) * 512 + 16 * grp + 2 * (n & 7)) * 2;
#pragma unroll
      for (int ks = 0; ks < 4; ++ks)
#pragma unroll
        for (int i = 0; i < 8; ++i) wm[ks * 8 + i] = __builtin_nontemporal_load((const u32*)(wp + (size_t)(32 * ks + i) * 6144));
    } else {
      xh = *(const u32*)(xin + b * 512 + c0);
    }
    u32 wqr[16];
    {
      const unsigned char* wp = (const unsigned char*)wq + (size_t)(128 * a + 32 * (n >> 3) + 8 * g) * 8192 + (x * 512 + idx * 16 + 2 * (n & 7)) * 2;
#pragma unroll
      for (int ks = 0; ks < 2; ++ks)
#pragma unroll
        for (int i = 0; i < 8; ++i) wqr[ks * 8 + i] = __builtin_nontemporal_load((const u32*)(wp + (size_t)(64 * ks + i) * 8192));
    }
    SCHED();
#pragma unroll
    for (int i = 0; i < 2; ++i) *(volatile LAS v4u*)(L + L_OPW + (i * 256 + a * 64 + lane) * 16) = v4u{SENT, SENT, SENT, SENT};
    if (a == 0) *(volatile LAS u32*)(L + L_FL + lane * 4) = 0;
    wgbar();
    auto abar = [&](u32 ph) { lflag(F_ABAR + 4 * a, ph); lwait4(F_ABAR, ph); };

    v4u Bw[4][4], Bd[8], Bo[4][4];
    const int fs = 7 * x + j;
    const unsigned char* wwp; int wws, wwi0, wwi1;
    if (!ismod) { wwp = (const unsigned char*)(n < 8 ? wg : wu) + (size_t)(128 * a + 64 * (g & 1) + 2 * (g >> 1)) * 3584 + (32 * fs + 4 * (n & 7)) * 2; wws = 4 * 3584; wwi0 = 3584; wwi1 = 16 * 3584; }
    else { wwp = (const unsigned char*)wmod + (size_t)(128 * a + 8 * g) * 6144 + (1024 + 64 * grp + 4 * n) * 2; wws = 32 * 6144; wwi0 = 6144; wwi1 = 2 * 6144; }
    const unsigned char* wop = (const unsigned char*)wo + (size_t)(x * 512 + 128 * a + 8 * g) * 1024 + (64 * j + 4 * n) * 2;
    auto load_w = [&](v2u* r, int ks) {
#pragma unroll
      for (int i = 0; i < 8; ++i) r[i] = __builtin_nontemporal_load((const v2u*)(wwp + ks * wws + (i >> 1) * wwi1 + (i & 1) * wwi0));
      SCHED();
    };
    auto load_o = [&](v2u* r, int ks) {
#pragma unroll
      for (int i = 0; i < 8; ++i) r[i] = __builtin_nontemporal_load((const v2u*)(wop + (((ks + b) & 3) * 32 + i) * 1024));
      SCHED();
    };
    auto all_o = [&]() {
      v2u r0[8], r1[8];
      load_o(r0, 0); load_o(r1, 1); tr4(r0, Bo[0]); SCHED(); load_o(r0, 2); tr4(r1, Bo[1]); SCHED(); load_o(r1, 3); tr4(r0, Bo[2]); SCHED(); tr4(r1, Bo[3]); SCHED();
    };
    v4u Ac;
    u32 tag;
    if (ismod) {
      v2u r[4][8];
#pragma unroll
      for (int ks = 0; ks < 4; ++ks) load_w(r[ks], ks);
      float xs[8];
#pragma unroll
      for (int q = 0; q < 4; ++q) { xs[2 * q] = blo(xv[q]); xs[2 * q + 1] = bhi(xv[q]); }
      const float mean = wsum(((xs[0] + xs[1]) + (xs[2] + xs[3])) + ((xs[4] + xs[5]) + (xs[6] + xs[7]))) * (1.f / 512.f);
      float sq = 0;
#pragma unroll
      for (int q = 0; q < 8; ++q) sq += (xs[q] - mean) * (xs[q] - mean);
      const float var = wsum(sq) * (1.f / 512.f);
      *(volatile LAS v2u*)(L + L_XST + a * 8) = v2u{f2u(rb(mean)), f2u(rcp(rb(__builtin_amdgcn_sqrtf(rb(rb(var) + 1e-5f)))))};
#pragma unroll
      for (int q = 0; q < 4; ++q) Ac[q] = cvtpk(silu(blo(cv[q])), silu(bhi(cv[q])));
      PIN(ep);
      tag = (u32)__builtin_amdgcn_readfirstlane(ep) * 1664525u + 1013904223u;
      SCHED();
      float p0 = 0, p1 = 0;
#pragma unroll
      for (int ks = 0; ks < 4; ++ks) {
        v4u B0, B1;
#pragma unroll
        for (int m = 0; m < 4; ++m) { B0[m] = plo(wm[ks * 8 + 2 * m], wm[ks * 8 + 2 * m + 1]); B1[m] = phi(wm[ks * 8 + 2 * m], wm[ks * 8 + 2 * m + 1]); }
        p0 += mfma(Ac, B0, v4f{0, 0, 0, 0})[ks]; p1 += mfma(Ac, B1, v4f{0, 0, 0, 0})[ks];
      }
      *(volatile LAS v2f*)(L + L_MP + lane * 32 + a * 8) = v2f{p0, p1};
      abar(1);
      {
        const int o = L_MP + (16 * g + (n & 7)) * 32;
        const v4f s0 = *(volatile LAS v4f*)(L + o), s1 = *(volatile LAS v4f*)(L + o + 16), c0_ = *(volatile LAS v4f*)(L + o + 256), c1_ = *(volatile LAS v4f*)(L + o + 272);
        const v2f st = *(volatile LAS v2f*)(L + L_XST + g * 8);
        const u32 sa = cvtpk((s0[0] + s0[2]) + (s1[0] + s1[2]), (s0[1] + s0[3]) + (s1[1] + s1[3]));
        const u32 ca = cvtpk((c0_[0] + c0_[2]) + (c1_[0] + c1_[2]), (c0_[1] + c0_[3]) + (c1_[1] + c1_[3]));
        const u32 c1 = cvtpk(1.f + blo(ca), 1.f + bhi(ca));
        const u32 d = cvtpk(blo(xh) - st[0], bhi(xh) - st[0]);
        const u32 nn = cvtpk(blo(d) * st[1], bhi(d) * st[1]);
        const u32 t = cvtpk(blo(nn) * blo(c1), bhi(nn) * bhi(c1));
        const u32 h = cvtpk(blo(t) + blo(sa), bhi(t) + bhi(sa));
        st8(rs, WS_H + (2 * a + (n >> 3)) * 8192 + grp * 256 + g * 64 + (n & 7) * 8, v2u{h, tag});
      }
      TS(1);
#pragma unroll
      for (int ks = 0; ks < 4; ++ks) tr4(r[ks], Bw[ks]);
#pragma unroll
      for (int q = 0; q < 8; ++q) Bd[q] = v4u{0, 0, 0, 0};
      if (WO_EARLY) all_o();
    } else {
      v2u r0[8], r1[8]; v4u rd[8];
      const unsigned char* dp = (const unsigned char*)wd + (size_t)(32 * fs + 8 * g) * 1024 + (128 * a + 8 * n) * 2;
      load_w(r0, 0); load_w(r1, 1); tr4(r0, Bw[0]); SCHED(); load_w(r0, 2); tr4(r1, Bw[1]); SCHED(); load_w(r1, 3); tr4(r0, Bw[2]); SCHED();
#pragma unroll
      for (int i = 0; i < 8; ++i) rd[i] = __builtin_nontemporal_load((const v4u*)(dp + i * 1024));
      SCHED();
      tr4(r1, Bw[3]); SCHED();
#pragma unroll
      for (int m = 0; m < 4; ++m)
#pragma unroll
        for (int q = 0; q < 4; ++q) { Bd[2 * q][m] = plo(rd[2 * m][q], rd[2 * m + 1][q]); Bd[2 * q + 1][m] = phi(rd[2 * m][q], rd[2 * m + 1][q]); }
#pragma unroll
      for (int q = 0; q < 8; ++q) PIN(Bd[q]);
      if (WO_EARLY) all_o();
      PIN(ep);
      tag = (u32)__builtin_amdgcn_readfirstlane(ep) * 1664525u + 1013904223u;
      Ac = v4u{0, 0, 0, 0};
      TS(1);
    }
    {
      v4u B[2][2];
#pragma unroll
      for (int ks = 0; ks < 2; ++ks)
#pragma unroll
        for (int m = 0; m < 4; ++m) { B[ks][0][m] = plo(wqr[ks * 8 + 2 * m], wqr[ks * 8 + 2 * m + 1]); B[ks][1][m] = phi(wqr[ks * 8 + 2 * m], wqr[ks * 8 + 2 * m + 1]); }
      PIN(B[0][0]); PIN(B[0][1]); PIN(B[1][0]); PIN(B[1][1]);
      const int k0 = 128 * a + 32 * (n & 3) + 8 * g;
      const int hoff = WS_H + x * 8192 + (k0 >> 4) * 256 + (n >> 2) * 64 + (k0 & 15) * 4;
      v4u h0, h1;
      SPIN(GSPIN) {
        h0 = ld16(rs, hoff); h1 = ld16(rs, hoff + 16);
        if (!__any(((h0.y ^ tag) | (h0.w ^ tag) | (h1.y ^ tag) | (h1.w ^ tag)) != 0)) break;
        __builtin_amdgcn_s_sleep(D1);
      }
      TS(2);
      const v4u A = {h0.x, h0.z, h1.x, h1.z};
      float pt[2];
#pragma unroll
      for (int cc = 0; cc < 2; ++cc) {
        const v4f d0 = mfma(A, B[0][cc], v4f{0, 0, 0, 0}), d1 = mfma(A, B[1][cc], v4f{0, 0, 0, 0});
        const float lo = d0[0] + d1[2], hi = d0[1] + d1[3], p = n < 8 ? lo : hi;
        pt[cc] = p + dppf<ROR8>(p);
      }
      *(volatile LAS v2f*)(L + L_QP + (g * 8 + (n & 7)) * 32 + a * 8) = v2f{pt[0], pt[1]};
      abar(2);
      if (a == 0) {
        const v4f s0 = *(volatile LAS v4f*)(L + L_QP + (lane & 31) * 32), s1 = *(volatile LAS v4f*)(L + L_QP + (lane & 31) * 32 + 16);
        const u32 qp = canon(cvtpk((s0[0] + s0[2]) + (s1[0] + s1[2]), (s0[1] + s0[3]) + (s1[1] + s1[3])));
        if (lane < 32) st4(rs, WS_Q + (x * 32 + idx) * 128 + lane * 4, qp);
      }
    }
    TS(3);
    {
      v4u qa[4];
      const int qoff = WS_Q + (x * 32 + (n & 3) * 8 + (g >> 1)) * 128 + b * 32 + (g & 1) * 16;
      __builtin_amdgcn_s_sleep(D2);
      SPIN(GSPIN) {
#pragma unroll
        for (int ks = 0; ks < 4; ++ks) qa[ks] = ld16(rs, qoff + ks * 256);
        if (!__any(umax(umax(max4(qa[0]), max4(qa[1])), umax(max4(qa[2]), max4(qa[3]))) == SENT)) break;
        __builtin_amdgcn_s_sleep(D1);
      }
      TS(4);
      lwait4(F_KDONE, 1);
      TS(5);
      float sc[8];
#pragma unroll
      for (int u = 0; u < 2; ++u) {
        v4f D[4];
#pragma unroll
        for (int t = 0; t < 4; ++t) {
          D[t] = v4f{0, 0, 0, 0};
#pragma unroll
          for (int ks = 0; ks < 4; ++ks) {
            const v4u kf = *(const LAS v4u*)(L + L_K + (128 * a + 64 * u + 16 * (n & 3) + 4 * t + (n >> 2)) * 256 + (((4 * ks + g) ^ n) * 16));
            D[t] = mfma(kf, qa[ks], D[t]);
          }
        }
#pragma unroll
        for (int r = 0; r < 4; r += 2) {
          const u32 w = cvtpk(sel4(n >> 2, D[0][r], D[1][r], D[2][r], D[3][r]), sel4(n >> 2, D[0][r + 1], D[1][r + 1], D[2][r + 1], D[3][r + 1]));
          const u32 w2 = cvtpk(blo(w) * 0.08838834764831845f, bhi(w) * 0.08838834764831845f);
          sc[4 * u + r] = blo(w2); sc[4 * u + r + 1] = bhi(w2);
        }
      }
      float m = fmaxf(fmaxf(fmaxf(sc[0], sc[1]), fmaxf(sc[2], sc[3])), fmaxf(fmaxf(sc[4], sc[5]), fmaxf(sc[6], sc[7])));
      m = fmaxf(m, dppf<ROR4>(m)); m = fmaxf(m, dppf<ROR8>(m)); m = max32(max16(m));
      *(volatile LAS float*)(L + L_MX + (n & 3) * 16 + a * 4) = m;
      abar(3);
      { const v4f t = *(volatile LAS v4f*)(L + L_MX + (n & 3) * 16); m = fmaxf(fmaxf(t[0], t[1]), fmaxf(t[2], t[3])); }
      float e[8];
#pragma unroll
      for (int q = 0; q < 8; ++q) e[q] = ex2((sc[q] - m) * LOG2E);
      *(volatile LAS v4u*)(L + L_P + ((((n >> 2) * 4 + a) * 4 + g) * 4 + (n & 3)) * 16) = v4u{cvtpk(e[0], e[1]), cvtpk(e[2], e[3]), cvtpk(e[4], e[5]), cvtpk(e[6], e[7])};
      lflag(F_PDONE + 4 * a, 1);
      float l = ((e[0] + e[1]) + (e[2] + e[3])) + ((e[4] + e[5]) + (e[6] + e[7]));
      l += dppf<ROR4>(l); l += dppf<ROR8>(l); l = sum32(sum16(l));
      if (lane < 4) st8(rs, WS_ML + cu * 128 + lane * 32 + a * 8, v2u{canon(f2u(m)), canon(f2u(l))});
    }
    TS(6);
    if (!WO_EARLY) all_o();
    TS(7);
    float Cc = 0, Bc = 0, xc0 = 0, xc1 = 0, gc0 = 0, gc1 = 0, ga0 = 0, ga1 = 0; u32 gbp = 0;
    const int ocol = 128 * (x >> 1) + 64 * j + 8 * (lane & 7) + 4 * (x & 1) + g;
    if (ismod) {
      float pt[4];
#pragma unroll
      for (int cc = 0; cc < 4; ++cc) {
        pt[cc] = 0;
#pragma unroll
        for (int ks = 0; ks < 4; ++ks) pt[cc] += mfma(Ac, Bw[ks][cc], v4f{0, 0, 0, 0})[ks];
        *(volatile LAS float*)(L + L_LP + (g * 64 + 4 * n + cc) * 16 + a * 4) = pt[cc];
      }
      abar(4);
      const v4f t0 = *(volatile LAS v4f*)(L + L_LP + (a * 64 + 2 * (lane & 31)) * 16), t1 = *(volatile LAS v4f*)(L + L_LP + (a * 64 + 2 * (lane & 31)) * 16 + 16);
      const v2u w = {cvtpk(sum4(t0), sum4(t1)), tag};
#pragma unroll
      for (int i = 0; i < 4; ++i) st8(rs, WS_MODL + ((2 * i + (lane >> 5)) * 4 + a) * 8192 + grp * 256 + (lane & 31) * 8, w);
    } else {
      v2u ga, sb, cb, gb;
      const int moff = WS_MODL + grp * 8192;
      SPIN(GSPIN) {
        ga = ld8(rs, moff + c0 * 4); sb = ld8(rs, moff + 2048 + c0 * 4); cb = ld8(rs, moff + 4096 + c0 * 4); gb = ld8(rs, moff + 6144 + (((ocol & 511) >> 1) * 8));
        if (!__any(((ga.y ^ tag) | (sb.y ^ tag) | (cb.y ^ tag) | (gb.y ^ tag)) != 0)) break;
        __builtin_amdgcn_s_sleep(8);
      }
      const u32 c1 = cvtpk(1.f + blo(cb.x), 1.f + bhi(cb.x));
      ga0 = blo(ga.x); ga1 = bhi(ga.x); gbp = (ocol & 1) ? (gb.x & 0xFFFF0000u) : (gb.x << 16);
      xc0 = blo(xh) * blo(c1); xc1 = bhi(xh) * bhi(c1); gc0 = ga0 * blo(c1); gc1 = ga1 * bhi(c1);
      *(volatile LAS u32*)(L + L_AP + a * 512 + (g * 4 + (n & 3)) * 16 + (n >> 2) * 4) = c1;
      *(volatile LAS u32*)(L + L_AP + a * 512 + 256 + (g * 4 + (n & 3)) * 16 + (n >> 2) * 4) = sb.x;
      const v4u A = *(volatile LAS v4u*)(L + L_AP + a * 512 + ((n >> 2) & 1) * 256 + (g * 4 + (n & 3)) * 16);
#pragma unroll
      for (int cc = 0; cc < 4; ++cc) {
        float p = 0;
#pragma unroll
        for (int ks = 0; ks < 4; ++ks) p += mfma(A, Bw[ks][cc], v4f{0, 0, 0, 0})[ks];
        *(volatile LAS float*)(L + L_LP + (g * 64 + 4 * n + cc) * 16 + a * 4) = p;
      }
      abar(4);
      const int f = (n >> 3) * 32 + 8 * g + (n & 7);
      Cc = sum4(*(volatile LAS v4f*)(L + L_LP + f * 16)); Bc = sum4(*(volatile LAS v4f*)(L + L_LP + 1024 + f * 16));
    }
    TS(8);
    float fr[4];
    {
      v2u t;
      SPIN(GSPIN) {
        t = ld8(rs, WS_ML + (grp * 8 + ((lane >> 2) & 7)) * 128 + a * 32 + (lane & 3) * 8);
        if (!__any(umax(t.x, t.y) == SENT)) break;
        __builtin_amdgcn_s_sleep(4);
      }
      const float m = u2f(t.x);
      float M = fmaxf(m, dppf<ROR4>(m)); M = fmaxf(M, dppf<ROR8>(M)); M = max16(M);
      const float w = ex2((m - M) * LOG2E);
      float l = u2f(t.y) * w;
      l += dppf<XOR1>(l); l += dppf<XOR2>(l); l += dppf<ROR4>(l); l += dppf<ROR8>(l); l = sum16(l);
      const float f = w / l;
      fr[0] = dppf<NBC + 0>(f); fr[1] = dppf<NBC + 4>(f); fr[2] = dppf<NBC + 8>(f); fr[3] = dppf<NBC + 12>(f);
      PIN(fr[0]); PIN(fr[1]); PIN(fr[2]); PIN(fr[3]);
      PIN(Cc); PIN(Bc); PIN(xc0); PIN(xc1); PIN(gc0); PIN(gc1); PIN(ga0); PIN(ga1); PIN(gbp);
    }
    TS(9);
    {
      lwait4(F_OPUB, 1);
      __builtin_amdgcn_s_sleep(D3);
      v4u oa[4];
      const int ooff = WS_OP + (grp * 8 + (n & 7)) * 1024 + (128 * a + 8 * g) * 2;
      SPIN(GSPIN) {
#pragma unroll
        for (int ks = 0; ks < 4; ++ks) oa[ks] = ld16(rs, ooff + ((ks + b) & 3) * 64);
        if (!__any(umax(umax(max4(oa[0]), max4(oa[1])), umax(max4(oa[2]), max4(oa[3]))) == SENT)) break;
      }
      TS(10);
      float t[4];
#pragma unroll
      for (int cc = 0; cc < 4; ++cc) {
        v4f ac = v4f{0, 0, 0, 0};
#pragma unroll
        for (int ks = 0; ks < 4; ++ks) ac = mfma(oa[ks], Bo[ks][cc], ac);
        t[cc] = (ac[0] * fr[0] + ac[1] * fr[1]) + (ac[2] * fr[2] + ac[3] * fr[3]);
      }
      swap16(t[0], t[1]); swap16(t[2], t[3]);
      const float y01 = t[0] + t[1], y23 = t[2] + t[3];
      *(volatile LAS float*)(L + L_YP + (4 * n + g) * 16 + a * 4) = lane < 32 ? y01 : y23;
      wgbar();
      const v4f y0 = *(volatile LAS v4f*)(L + L_YP + (lane & 31) * 32), y1 = *(volatile LAS v4f*)(L + L_YP + (lane & 31) * 32 + 16);
      st8(rs, WS_Y + (((2 * a + (lane >> 5)) * 4 + b) * 8 + x) * 2048 + j * 256 + (lane & 31) * 8, v2u{cvtpk(sum4(y0), sum4(y1)), tag});
    }
    TS(11);
    if (!ismod) {
      __builtin_amdgcn_s_sleep(D4);
      v4u y[4];
      const int yoff = WS_Y + (grp * 8 + (lane >> 5)) * 2048 + (128 * a + 4 * (lane & 31)) * 4;
      SPIN(GSPIN) {
#pragma unroll
        for (int i = 0; i < 4; ++i) y[i] = ld16(rs, yoff + i * 4096);
        const u32 bad = ((y[0].y ^ tag) | (y[0].w ^ tag)) | ((y[1].y ^ tag) | (y[1].w ^ tag)) | ((y[2].y ^ tag) | (y[2].w ^ tag)) | ((y[3].y ^ tag) | (y[3].w ^ tag));
        if (!__any(bad != 0)) break;
      }
      TS(12);
      float v0 = (blo(y[0].x) + blo(y[1].x)) + (blo(y[2].x) + blo(y[3].x)), v1 = (bhi(y[0].x) + bhi(y[1].x)) + (bhi(y[2].x) + bhi(y[3].x));
      float v2 = (blo(y[0].z) + blo(y[1].z)) + (blo(y[2].z) + blo(y[3].z)), v3 = (bhi(y[0].z) + bhi(y[1].z)) + (bhi(y[2].z) + bhi(y[3].z));
      swap32(v0, v2); swap32(v1, v3);
      const float ya = v0 + v2, yb = v1 + v3;
      const u32 hp = cvtpk(ya * gc0 + xc0, yb * gc1 + xc1);
      const v4u A = {hp, dppu<SHL + 4>(hp), dppu<SHL + 8>(hp), dppu<SHL + 12>(hp)};
      const float xa = ya * ga0 + blo(xh), xb = yb * ga1 + bhi(xh);
      float pt[4];
#pragma unroll
      for (int cc = 0; cc < 4; ++cc) {
        v4f d[4];
#pragma unroll
        for (int ks = 0; ks < 4; ++ks) d[ks] = mfma(A, Bw[ks][cc], v4f{0, 0, 0, 0});
        pt[cc] = (d[0][0] + d[1][1]) + (d[2][2] + d[3][3]);
      }
      const float s1 = wsum(xa + xb), s2 = wsum(xa * xa + xb * xb);
      *(volatile LAS v2f*)(L + L_ST + a * 8) = v2f{s1, s2};
      if (j < 2) *(volatile LAS v2f*)(L + L_X1 + c0 * 4) = v2f{xa, xb};
      if (g == 0) {
#pragma unroll
        for (int cc = 0; cc < 4; ++cc) *(volatile LAS float*)(L + L_GP + (4 * n + cc) * 16 + a * 4) = pt[cc];
      }
      wgbar();
      const v4f t = *(volatile LAS v4f*)(L + L_GP + ((n >> 3) * 32 + 8 * g + (n & 7)) * 16);
      const v4f q0 = *(volatile LAS v4f*)(L + L_ST), q1 = *(volatile LAS v4f*)(L + L_ST + 16);
      const float mu = ((q0[0] + q0[2]) + (q1[0] + q1[2])) * (1.f / 512.f), e2 = ((q0[1] + q0[3]) + (q1[1] + q1[3])) * (1.f / 512.f);
      const float rstd = __builtin_amdgcn_rsqf(fmaxf(e2 - mu * mu, 0.f) + 1e-5f);
      const float val = (sum4(t) - mu * Cc) * rstd + Bc;
      const float act = silu(val) * dppf<ROR8>(val);
      const u32 ap = cvtpk(act, dppf<SHL + 1>(act));
      const v4u A2 = {ap, dppu<SHL + 2>(ap), dppu<SHL + 4>(ap), dppu<SHL + 6>(ap)};
      float z[8];
#pragma unroll
      for (int cc = 0; cc < 8; ++cc) z[cc] = mfma(A2, Bd[cc], v4f{0, 0, 0, 0})[0];
      if (g == 0) {
        const int zo = WS_Z + (b * 56 + fs) * 2048 + a * 512 + n * 16;
        st16(rs, zo, v4u{cvtpk(z[0], z[1]), tag, cvtpk(z[2], z[3]), tag});
        st16(rs, zo + 256, v4u{cvtpk(z[4], z[5]), tag, cvtpk(z[6], z[7]), tag});
      }
      TS(13);
      if (a == 0 && j < 2) {
        const float x1 = *(volatile LAS float*)(L + L_X1 + ocol * 4);
        if (bid == 0 && lane == 0) st4(rs, WS_EPOCH, tag);
        __builtin_amdgcn_s_sleep(D5);
        v4u zz[7];
        const int zoff = WS_Z + (b * 56 + (lane >> 3)) * 2048 + (2 * x + j) * 128 + (lane & 7) * 16;
        SPIN(GSPIN) {
          u32 bad = 0;
#pragma unroll
          for (int i = 0; i < 7; ++i) zz[i] = ld16(rs, zoff + i * 16384);
#pragma unroll
          for (int i = 0; i < 7; ++i) bad |= (zz[i].y ^ tag) | (zz[i].w ^ tag);
          if (!__any(bad != 0)) break;
        }
        TS(14);
        float w0 = 0, w1 = 0, w2 = 0, w3 = 0;
#pragma unroll
        for (int i = 0; i < 7; ++i) { w0 += blo(zz[i].x); w1 += bhi(zz[i].x); w2 += blo(zz[i].z); w3 += bhi(zz[i].z); }
        swap32(w0, w2); swap32(w1, w3);
        float r0 = w0 + w2, r1 = w1 + w3;
        swap16(r0, r1);
        float r = r0 + r1;
        r += dppf<ROR8>(r);
        if (n < 8) out[b * 512 + ocol] = (u16)cvtpk(x1 + u2f(gbp) * r, 0.f);
      }
    }
  }
  TS(15);
}

__global__ void padk(unsigned char* ws) {
  const u64 t0 = *(volatile u64*)(ws + (8 << 20));
  SPIN(100000) { if (__builtin_amdgcn_s_memrealtime() - t0 >= 6000) break; __builtin_amdgcn_s_sleep(1); }
}
extern "C" void kernel_launch(void* const* d_in, const int* in_sizes, int n_in, void* d_out, int out_size, void* d_ws, size_t ws_size,
                              hipStream_t stream) {
  fused_kernel<<<256, 512, 0, stream>>>((const u16*)d_in[0], (const u16*)d_in[1], (const u16*)d_in[2], (const u16*)d_in[3], (const u16*)d_in[4],
                                        (const u16*)d_in[6], (const u16*)d_in[7], (const u16*)d_in[8], (const u16*)d_in[9], (const u16*)d_in[10],
                                        (u16*)d_out, (unsigned char*)d_ws);
  padk<<<1, 64, 0, stream>>>((unsigned char*)d_ws);
}
```

```cpp
#include <hip/hip_runtime.h>
#include <hip/hip_bf16.h>
#include <stdint.h>

typedef unsigned u32;
typedef unsigned short u16;
typedef unsigned long long u64;
typedef u32 v4u __attribute__((ext_vector_type(4)));
typedef u32 v2u __attribute__((ext_vector_type(2)));
typedef float v4f __attribute__((ext_vector_type(4)));
typedef float v2f __attribute__((ext_vector_type(2)));
typedef __bf16 v2b __attribute__((ext_vector_type(2)));
typedef short v8s __attribute__((ext_vector_type(8)));
#define LAS __attribute__((address_space(3)))
#define DEV __device__ __forceinline__

#define WH 8
#define WL 6
#define FE 2
#define DE 0
#define WV 1
#define NE 0
#define D1 4
#define D3 0
#define D4 0
#define D5 8
#define APRIO 3
#define LPRIO 3

constexpr int WS_EPOCH = 0;
constexpr int WS_RDY = 4096;
constexpr int WS_SAT = WS_RDY + 32768;
constexpr int WS_Q = WS_SAT + 131072;
constexpr int WS_MODL = WS_Q + 65536;
constexpr int WS_ML = WS_MODL + 262144;
constexpr int WS_OP = WS_ML + 32768;
constexpr int WS_Y = WS_OP + 262144;
constexpr int WS_Z = WS_Y + 262144;
constexpr int WS_END = WS_Z + 4 * 56 * 1024;
constexpr int WS_STAMP = 4 << 20;
constexpr int OOB = 0x7FFFFF00;

constexpr int L_K = 0;
constexpr int L_P = 131072;
constexpr int L_OPW = L_P + 4096;
constexpr int L_FL = L_OPW + 8192;
constexpr int F_KDONE = L_FL, F_PDONE = L_FL + 16, F_ABAR = L_FL + 48;
constexpr int L_DP = L_FL + 256;
constexpr int L_Q = L_DP + 4096;
constexpr int L_MX = L_Q + 1088;
constexpr int L_PC = L_MX + 64;
constexpr int L_YP = L_PC + 4096;
constexpr int L_GP = L_YP + 2048;
constexpr int L_ST = L_GP + 4096;
constexpr int L_TOTAL = L_ST + 64;

constexpr u32 SENT = 0xFFFFFFFFu;
constexpr int GSPIN = 1 << 16, LSPIN = 1 << 20;

DEV float u2f(u32 u) { return __builtin_bit_cast(float, u); }
DEV u32 f2u(float f) { return __builtin_bit_cast(u32, f); }
DEV u32 cvtpk(float a, float b) { v2f f = {a, b}; return __builtin_bit_cast(u32, __builtin_convertvector(f, v2b)); }
DEV float blo(u32 p) { return u2f(p << 16); }
DEV float bhi(u32 p) { return u2f(p & 0xFFFF0000u); }
DEV float rb(float f) { return blo(cvtpk(f, f)); }
DEV u32 canon(u32 u) { return u < 0xFFFFFFFEu ? u : 0xFFFFFFFEu; }
DEV float ex2(float v) { return __builtin_amdgcn_exp2f(v); }
DEV float rcp(float v) { return __builtin_amdgcn_rcpf(v); }
constexpr float LOG2E = 1.4426950408889634f;
DEV float silu(float v) { return v * rcp(1.f + ex2(-LOG2E * v)); }
template <int C> DEV u32 dppu(u32 v) { return (u32)__builtin_amdgcn_mov_dpp((int)v, C, 0xf, 0xf, true); }
template <int C> DEV float dppf(float v) { return u2f(dppu<C>(f2u(v))); }
constexpr int XOR1 = 0xB1, XOR2 = 0x4E, ROR4 = 0x124, ROR8 = 0x128, SHL = 0x100, NBC = 0x150;
DEV void swap32(float& a, float& b) { auto r = __builtin_amdgcn_permlane32_swap(f2u(a), f2u(b), false, false); a = u2f(r[0]); b = u2f(r[1]); }
DEV void swap16(float& a, float& b) { auto r = __builtin_amdgcn_permlane16_swap(f2u(a), f2u(b), false, false); a = u2f(r[0]); b = u2f(r[1]); }
DEV float sum16(float v) { float a = v, b = v; swap16(a, b); return a + b; }
DEV float sum32(float v) { float a = v, b = v; swap32(a, b); return a + b; }
DEV float max16(float v) { float a = v, b = v; swap16(a, b); return fmaxf(a, b); }
DEV float max32(float v) { float a = v, b = v; swap32(a, b); return fmaxf(a, b); }
DEV float wsum(float v) { v += dppf<XOR1>(v); v += dppf<XOR2>(v); v += dppf<ROR4>(v); v += dppf<ROR8>(v); return sum32(sum16(v)); }
DEV v4f mfma(v4u a, v4u b, v4f c) { return __builtin_amdgcn_mfma_f32_16x16x32_bf16(__builtin_bit_cast(v8s, a), __builtin_bit_cast(v8s, b), c, 0, 0, 0); }
DEV u32 plo(u32 k0, u32 k1) { return __builtin_amdgcn_perm(k1, k0, 0x05040100u); }
DEV u32 phi(u32 k0, u32 k1) { return __builtin_amdgcn_perm(k1, k0, 0x07060302u); }
DEV float sel4(int g, float a, float b, float c, float d) { float lo = (g & 1) ? b : a, hi = (g & 1) ? d : c; return (g & 2) ? hi : lo; }
DEV u32 umax(u32 a, u32 b) { return a > b ? a : b; }
DEV u32 max4(v4u v) { return umax(umax(v.x, v.y), umax(v.z, v.w)); }
DEV float sum4(v4f v) { return (v[0] + v[1]) + (v[2] + v[3]); }
#define CBAR() asm volatile("" ::: "memory")
#define SCHED() __builtin_amdgcn_sched_barrier(0)
#define PIN(v) asm volatile("" : "+v"(v))
#define SPIN(N) _Pragma("unroll 1") for (int sp = 0; sp < (N); ++sp)

typedef __amdgpu_buffer_rsrc_t rsrc_t;
DEV v4u ld16(rsrc_t rs, int off) { CBAR(); v4u v = __builtin_amdgcn_raw_buffer_load_b128(rs, off, 0, 16); CBAR(); return v; }
DEV v2u ld8(rsrc_t rs, int off) { CBAR(); v2u v = __builtin_amdgcn_raw_buffer_load_b64(rs, off, 0, 16); CBAR(); return v; }
DEV u32 ld4(rsrc_t rs, int off) { CBAR(); u32 v = __builtin_amdgcn_raw_buffer_load_b32(rs, off, 0, 16); CBAR(); return v; }
DEV void st16(rsrc_t rs, int off, v4u v) { CBAR(); __builtin_amdgcn_raw_buffer_store_b128(v, rs, off, 0, 16); CBAR(); }
DEV void st8(rsrc_t rs, int off, v2u v) { CBAR(); __builtin_amdgcn_raw_buffer_store_b64(v, rs, off, 0, 16); CBAR(); }
DEV void st4(rsrc_t rs, int off, u32 v) { CBAR(); __builtin_amdgcn_raw_buffer_store_b32(v, rs, off, 0, 16); CBAR(); }

DEV void dma16(const void* sbase, u32 voff, u32 ldsdst) {
  u32 keep;
  asm volatile("s_mov_b32 %0, m0\n\ts_mov_b32 m0, %3\n\ts_nop 0\n\tglobal_load_lds_dwordx4 %1, %2 nt\n\ts_mov_b32 m0, %0" : "=&s"(keep) : "v"(voff), "s"(sbase), "s"(ldsdst) : "memory");
}
template <int N> DEV void vmwait() { asm volatile("s_waitcnt vmcnt(%0)" ::"i"(N) : "memory"); }
DEV void wgbar() { asm volatile("s_waitcnt lgkmcnt(0)\n\ts_barrier" ::: "memory"); }

#define TS(slot) do {} while (0)

DEV void tr4(const v2u* r, v4u* B) {
#pragma unroll
  for (int m = 0; m < 4; ++m) {
    B[0][m] = plo(r[2 * m].x, r[2 * m + 1].x); B[1][m] = phi(r[2 * m].x, r[2 * m + 1].x);
    B[2][m] = plo(r[2 * m].y, r[2 * m + 1].y); B[3][m] = phi(r[2 * m].y, r[2 * m + 1].y);
  }
  PIN(B[0]); PIN(B[1]); PIN(B[2]); PIN(B[3]);
}

__global__ void __launch_bounds__(512, 2)
fused_kernel(const u16* __restrict__ xin, const u16* __restrict__ kc, const u16* __restrict__ vc, const u16* __restrict__ wq,
             const u16* __restrict__ wo, const u16* __restrict__ wg, const u16* __restrict__ wu, const u16* __restrict__ wd,
             const u16* __restrict__ cin, const u16* __restrict__ wmod, u16* __restrict__ out, unsigned char* ws) {
  __shared__ __attribute__((aligned(16))) unsigned char lds_[L_TOTAL];
  LAS unsigned char* L = (LAS unsigned char*)lds_;
  const int tid = threadIdx.x, lane = tid & 63, wave = __builtin_amdgcn_readfirstlane(tid >> 6);
  const int bid = blockIdx.x, x = bid & 7, idx = bid >> 3, b = idx >> 3, j = idx & 7;
  const int n = lane & 15, g = lane >> 4;
  const int bp = b >> 1, r8 = (b & 1) * 4 + (j & 3);
  const bool isW = j < 4, isM = !isW && r8 == 7, isF = !isW && !isM;
  const bool isQ = isW && bp == 0, isL = isW && bp == 1 && r8 < 4;
  const int grp = x * 4 + b, cu = grp * 8 + j;
  const int oprow = WS_OP + (x * 2 + bp) * 16384 + ((b & 1) * 8 + j) * 64;
  const rsrc_t rs = __builtin_amdgcn_make_buffer_rsrc(ws, 0, WS_END, 0x00020000);
  auto lflag = [&](int off, u32 v) { *(volatile LAS u32*)(L + off) = v; };
  auto lwait4 = [&](int off, u32 v) {
    SPIN(LSPIN) { v4u f = *(volatile LAS v4u*)(L + off); if (min(min(f.x, f.y), min(f.z, f.w)) >= v) break; __builtin_amdgcn_s_sleep(1); }
  };
  TS(0);

  if (wave < 4) {
    const int s = wave;
    const size_t kvoff = ((size_t)(b * 8 + x) << 20) + (size_t)s * 1024;
    auto co = [&](int i) { return (size_t)i * 32768 + (size_t)((j + i) & 7) * 4096; };
    const unsigned char* kp = (const unsigned char*)kc + kvoff;
    const unsigned char* vp = (const unsigned char*)vc + kvoff + lane * 16;
    const u32 lk = (u32)(uintptr_t)L + L_K + s * 1024;
    auto win = [&]() { if (WL != WH && !isF) vmwait<WL>(); vmwait<WH>(); };
    v4u va[32];
    v4u T[4][8];
    const bool first = isQ || isM;
    if (first) __builtin_amdgcn_s_barrier();
    va[0] = __builtin_nontemporal_load((const v4u*)(vp + co(0)));
    SCHED();
#pragma unroll
    for (int i = 0; i < 32; ++i) {
      dma16(kp + co(i), g * 256 + ((n ^ ((i & 3) | (g << 2))) * 16), lk + i * 4096);
      if (i == WL - 2 && !first) __builtin_amdgcn_s_barrier();
      if (i >= WL - 1) win();
    }
    lflag(F_KDONE + 4 * s, 1);
    TS(1);
#pragma unroll
    for (int i = 1; i < 32; ++i) {
      va[i] = __builtin_nontemporal_load((const v4u*)(vp + co(i)));
      SCHED();
      if (i == 31) break;
      win();
      if (i == WH) lflag(F_KDONE + 4 * s, 2);
      const int c = i - WH;
      if (c >= 0 && (c & 1)) {
        const int G = c >> 3, m = (c & 7) >> 1;
#pragma unroll
        for (int q = 0; q < 4; ++q) { u32 t0 = plo(va[c - 1][q], va[c][q]), t1 = phi(va[c - 1][q], va[c][q]); PIN(t0); PIN(t1); T[G][2 * q][m] = t0; T[G][2 * q + 1][m] = t1; }
      }
      SCHED();
    }
    lflag(F_KDONE + 4 * s, 3);
    TS(2);
    lwait4(F_PDONE, 1);
    TS(3);
    v4u pa[4];
#pragma unroll
    for (int G = 0; G < 4; ++G) pa[G] = *(volatile LAS v4u*)(L + L_P + (((s * 4 + G) * 4 + g) * 4 + (n >> 2)) * 16);
    v4f acc[8];
#pragma unroll
    for (int d = 0; d < 8; ++d) acc[d] = v4f{0, 0, 0, 0};
    constexpr int CDONE = (31 - WH) & ~1;
#pragma unroll
    for (int G = 0; G < 4; ++G) {
#pragma unroll
      for (int m = 0; m < 4; ++m) {
        const int c = G * 8 + 2 * m;
        if (c >= CDONE) {
#pragma unroll
          for (int q = 0; q < 4; ++q) { T[G][2 * q][m] = plo(va[c][q], va[c + 1][q]); T[G][2 * q + 1][m] = phi(va[c][q], va[c + 1][q]); }
          if (c < 30) SCHED();
        }
      }
#pragma unroll
      for (int d = 0; d < 8; ++d) acc[d] = mfma(pa[G], T[G][d], acc[d]);
      if (G < 3) SCHED();
    }
    *(volatile LAS v4u*)(L + L_OPW + s * 2048 + lane * 32) = v4u{canon(f2u(acc[0][0])), canon(f2u(acc[1][0])), canon(f2u(acc[2][0])), canon(f2u(acc[3][0]))};
    *(volatile LAS v4u*)(L + L_OPW + s * 2048 + lane * 32 + 16) = v4u{canon(f2u(acc[4][0])), canon(f2u(acc[5][0])), canon(f2u(acc[6][0])), canon(f2u(acc[7][0]))};
    TS(4);
    {
      v2u o0, o1, o2, o3;
      SPIN(LSPIN) {
        o0 = *(volatile LAS v2u*)(L + L_OPW + 0 * 2048 + s * 512 + lane * 8); o1 = *(volatile LAS v2u*)(L + L_OPW + 1 * 2048 + s * 512 + lane * 8);
        o2 = *(volatile LAS v2u*)(L + L_OPW + 2 * 2048 + s * 512 + lane * 8); o3 = *(volatile LAS v2u*)(L + L_OPW + 3 * 2048 + s * 512 + lane * 8);
        u32 mx = umax(umax(umax(o0.x, o0.y), umax(o1.x, o1.y)), umax(umax(o2.x, o2.y), umax(o3.x, o3.y)));
        if (!__any(mx == SENT)) break;
      }
      const float e = (u2f(o0.x) + u2f(o1.x)) + (u2f(o2.x) + u2f(o3.x)), o = (u2f(o0.y) + u2f(o1.y)) + (u2f(o2.y) + u2f(o3.y));
      st4(rs, oprow + s * 4096 + g * 1024 + n * 4, canon(cvtpk(e, o)));
    }
    TS(5);
  } else {
    const int a = wave - 4;
    const int fs = 7 * x + r8;
    __builtin_amdgcn_s_setprio(APRIO);
    {
      int po;
      if (a == 0) po = oprow + g * 4096 + (n >> 2) * 1024 + (n & 3) * 16;
      else if (a == 3) po = lane < 8 ? WS_ML + (grp * 4 + (lane >> 1)) * 256 + j * 32 + (lane & 1) * 16 : OOB;
      else if (isW) po = WS_Y + (((lane >> 3) * 4 + 2 * bp + a - 1) * 8 + x) * 1024 + r8 * 128 + (lane & 7) * 16;
      else if (isF) po = WS_Z + ((2 * bp + a - 1) * 56 + fs) * 1024 + lane * 16;
      else po = OOB;
      st16(rs, po, v4u{SENT, SENT, SENT, SENT});
    }
    u32 ep = ld4(rs, WS_EPOCH);
    u32 tag;
    auto ready = [&]() {
      TS(16);
      PIN(ep);
      tag = (u32)__builtin_amdgcn_readfirstlane(ep) * 1664525u + 1013904223u;
      TS(17);
      st4(rs, lane < 8 ? WS_RDY + lane * 4096 + cu * 16 + a * 4 : OOB, tag);
    };
    auto ldsinit = [&]() {
#pragma unroll
      for (int i = 0; i < 2; ++i) *(volatile LAS v4u*)(L + L_OPW + (i * 256 + a * 64 + lane) * 16) = v4u{SENT, SENT, SENT, SENT};
      if (a == 0) *(volatile LAS u32*)(L + L_FL + lane * 4) = 0;
      wgbar();
    };
    auto abar = [&](u32 ph) { lflag(F_ABAR + 4 * a, ph); lwait4(F_ABAR, ph); };

    v4u Bw[4][4], Bd[8];
    const int nn = n ^ (2 * bp);
    const int fcol = 128 * a + 64 * (g >> 1) + 8 * (nn & 7) + 4 * (g & 1);
    const unsigned char* wwp; int wws, wwi0, wwi1;
    if (isW) { wwp = (const unsigned char*)wo + (size_t)(x * 512 + 128 * a + 8 * g) * 1024 + (64 * r8 + 4 * n) * 2; wws = 32 * 1024; wwi0 = 1024; wwi1 = 4 * 1024; }
    else { wwp = (const unsigned char*)(n < 8 ? wg : wu) + (size_t)(128 * a + 64 * (g >> 1) + 4 * (g & 1)) * 3584 + (32 * fs + 4 * (n & 7)) * 2; wws = 8 * 3584; wwi0 = 3584; wwi1 = 32 * 3584; }
    auto load_w = [&](v2u* r, int sl) {
      const unsigned char* p = wwp + (sl ^ (2 * bp)) * wws;
#pragma unroll
      for (int i = 0; i < 8; ++i) r[i] = __builtin_nontemporal_load((const v2u*)(p + (i >> 2) * wwi1 + (i & 3) * wwi0));
      SCHED();
    };
    auto load_d = [&](v4u* rd) {
      const unsigned char* dp = (const unsigned char*)wd + (size_t)(32 * fs + 8 * g) * 1024 + (128 * a + 8 * n) * 2;
#pragma unroll
      for (int i = 0; i < 8; ++i) rd[i] = __builtin_nontemporal_load((const v4u*)(dp + i * 1024));
      SCHED();
    };
    auto tr8 = [&](const v4u* rd, v4u* B) {
#pragma unroll
      for (int m = 0; m < 4; ++m)
#pragma unroll
        for (int q = 0; q < 4; ++q) { B[2 * q][m] = plo(rd[2 * m][q], rd[2 * m + 1][q]); B[2 * q + 1][m] = phi(rd[2 * m][q], rd[2 * m + 1][q]); }
#pragma unroll
      for (int q = 0; q < 8; ++q) PIN(B[q]);
    };
    v2u xf = {0, 0};
    v4u mga, msb, mcb, mgb;

    if (isQ || isL || isM) {
      const int e = 2 * x + bp;
      const int wst = isQ ? 8192 : 6144;
      const int col0 = isQ ? x * 512 + 64 * r8 : (isM ? 64 * e : 1024 + 64 * (4 * x + r8));
      const unsigned char* wp = (const unsigned char*)(isQ ? wq : wmod) + (size_t)(128 * a + 16 * (n >> 3) + 4 * g) * wst + (col0 + 8 * (n & 7)) * 2;
      const int ak = 128 * a + 16 * (n & 3) + 4 * g;
      const v2u c0 = *(const v2u*)(cin + (n >> 2) * 512 + ak), c1 = *(const v2u*)(cin + (n >> 2) * 512 + ak + 64);
      const v4u xv = *(const v4u*)(xin + a * 512 + lane * 8);
      const u32 xh = *(const u32*)(xin + a * 512 + 64 * (e & 7) + 2 * (lane & 31));
      SCHED();
      v4u r[2][8];
#pragma unroll
      for (int kq = 0; kq < 2; ++kq)
#pragma unroll
        for (int i = 0; i < 8; ++i) r[kq][i] = __builtin_nontemporal_load((const v4u*)(wp + (size_t)(64 * (i >> 2) + 32 * kq + (i & 3)) * wst));
      SCHED();
      ldsinit();
      ready();
      v4u A = {cvtpk(silu(blo(c0.x)), silu(bhi(c0.x))), cvtpk(silu(blo(c0.y)), silu(bhi(c0.y))), cvtpk(silu(blo(c1.x)), silu(bhi(c1.x))), cvtpk(silu(blo(c1.y)), silu(bhi(c1.y)))};
      u32 nrm;
      {
        float xs[8];
#pragma unroll
        for (int q = 0; q < 4; ++q) { xs[2 * q] = blo(xv[q]); xs[2 * q + 1] = bhi(xv[q]); }
        const float mean = wsum(((xs[0] + xs[1]) + (xs[2] + xs[3])) + ((xs[4] + xs[5]) + (xs[6] + xs[7]))) * (1.f / 512.f);
        float sq = 0;
#pragma unroll
        for (int q = 0; q < 8; ++q) sq += (xs[q] - mean) * (xs[q] - mean);
        const float var = wsum(sq) * (1.f / 512.f);
        const float mb = rb(mean), rs_ = rcp(rb(__builtin_amdgcn_sqrtf(rb(rb(var) + 1e-5f))));
        const u32 d = cvtpk(blo(xh) - mb, bhi(xh) - mb);
        nrm = cvtpk(blo(d) * rs_, bhi(d) * rs_);
        PIN(nrm); PIN(A);
      }
      SCHED();
      v4u B0[8], B1[8];
      tr8(r[0], B0); SCHED(); TS(18); tr8(r[1], B1); SCHED();
      TS(1);
      if (isQ) {
        const int so = WS_SAT + x * 16384 + (n >> 2) * 2048 + ak * 4;
        v4u s0, s1, t0, t1;
        SPIN(GSPIN) {
          const v4u rd = ld16(rs, WS_RDY + x * 4096 + (64 * a + lane) * 16);
          if (!__any(((rd.x ^ tag) | (rd.y ^ tag) | (rd.z ^ tag) | (rd.w ^ tag)) != 0)) break;
          __builtin_amdgcn_s_sleep(D1);
        }
        TS(7);
        SPIN(GSPIN) {
          s0 = ld16(rs, so); s1 = ld16(rs, so + 256); t0 = ld16(rs, so + 8192); t1 = ld16(rs, so + 8192 + 256);
          const u32 bad = ((s0.y ^ tag) | (s0.w ^ tag) | (s1.y ^ tag) | (s1.w ^ tag)) | ((t0.y ^ tag) | (t0.w ^ tag) | (t1.y ^ tag) | (t1.w ^ tag));
          if (!__any(bad != 0)) break;
          __builtin_amdgcn_s_sleep(D1);
        }
        A = v4u{cvtpk(blo(t0.x) + blo(s0.x), bhi(t0.x) + bhi(s0.x)), cvtpk(blo(t0.z) + blo(s0.z), bhi(t0.z) + bhi(s0.z)),
                cvtpk(blo(t1.x) + blo(s1.x), bhi(t1.x) + bhi(s1.x)), cvtpk(blo(t1.z) + blo(s1.z), bhi(t1.z) + bhi(s1.z))};
      }
      TS(2);
      const v4u A2 = {dppu<XOR2>(A[0]), dppu<XOR2>(A[1]), dppu<XOR2>(A[2]), dppu<XOR2>(A[3])};
      v4f d[8];
#pragma unroll
      for (int cc = 0; cc < 8; ++cc) d[cc] = mfma(A, B0[cc], v4f{0, 0, 0, 0});
#pragma unroll
      for (int cc = 0; cc < 8; ++cc) d[cc] = mfma(A2, B1[cc], d[cc]);
      v4f p;
#pragma unroll
      for (int cc = 0; cc < 4; ++cc) { const float own = n < 8 ? d[cc][0] : d[cc + 4][1], oth = n < 8 ? d[cc + 4][0] : d[cc][1]; p[cc] = own + dppf<ROR8>(oth); }
      *(volatile LAS v4f*)(L + L_DP + a * 1024 + g * 256 + (n & 7) * 32 + (n >> 3) * 16) = p;
      abar(1);
      const int ro = L_DP + a * 256 + (lane & 31) * 8;
      const v2f q0 = *(volatile LAS v2f*)(L + ro), q1 = *(volatile LAS v2f*)(L + ro + 1024), q2 = *(volatile LAS v2f*)(L + ro + 2048), q3 = *(volatile LAS v2f*)(L + ro + 3072);
      u32 w = cvtpk((q0[0] + q1[0]) + (q2[0] + q3[0]), (q0[1] + q1[1]) + (q2[1] + q3[1]));
      if (isQ) {
        st8(rs, lane < 32 ? WS_Q + (x * 4 + a) * 2048 + r8 * 256 + lane * 8 : OOB, v2u{w, tag});
        if (bid == 0 && a == 0) st4(rs, lane == 0 ? WS_EPOCH : OOB, tag);
      } else {
        if (isM && e >= 8) { const u32 c1_ = cvtpk(1.f + blo(w), 1.f + bhi(w)); w = cvtpk(blo(nrm) * blo(c1_), bhi(nrm) * bhi(c1_)); }
        const int rst = isM ? 16384 : 32768;
        const int o = (isM ? WS_SAT + (e >> 3) * 8192 + a * 2048 + (e & 7) * 256 : WS_MODL + a * 8192 + (4 * x + r8) * 256) + (lane >> 5) * rst + (lane & 31) * 8;
#pragma unroll
        for (int i = 0; i < 4; ++i) st8(rs, o + 2 * i * rst, v2u{w, tag});
      }
      TS(3);
    } else if (isF) {
      xf = *(const v2u*)(xin + (2 * bp + (n >> 3)) * 512 + fcol);
      v2u r0[8], r1[8]; v4u rd[8];
      if (FE >= 2) { load_w(r0, 0); load_w(r1, 1); }
      ldsinit();
      ready();
      if (FE >= 2) { tr4(r0, Bw[0]); SCHED(); if (FE >= 4) load_w(r0, 2); tr4(r1, Bw[1]); SCHED(); }
      if (FE >= 4) { load_w(r1, 3); tr4(r0, Bw[2]); SCHED(); }
      if (DE) load_d(rd);
      if (FE >= 4) { tr4(r1, Bw[3]); SCHED(); }
      if (DE) tr8(rd, Bd);
      TS(1);
    } else {
      v2u r0[8], r1[8];
      if (NE) { load_w(r0, 0); load_w(r1, 1); }
      ldsinit();
      ready();
      if (NE) { tr4(r0, Bw[0]); SCHED(); load_w(r0, 2); tr4(r1, Bw[1]); SCHED(); load_w(r1, 3); tr4(r0, Bw[2]); SCHED(); tr4(r1, Bw[3]); SCHED(); }
    }
    {
      v2u qv;
      lwait4(F_KDONE, 1);
      SPIN(GSPIN) {
        qv = ld8(rs, WS_Q + grp * 2048 + a * 512 + lane * 8);
        if (!__any(qv.y != tag)) break;
        __builtin_amdgcn_s_sleep(D1);
      }
      *(volatile LAS u32*)(L + L_Q + a * 272 + lane * 4) = qv.x;
      TS(4);
      abar(2);
      v4u qa[4];
#pragma unroll
      for (int ks = 0; ks < 4; ++ks) qa[ks] = *(volatile LAS v4u*)(L + L_Q + (n & 3) * 272 + ks * 64 + g * 16);
      lwait4(F_KDONE, 2);
      TS(5);
      float sc[8];
#pragma unroll
      for (int u = 0; u < 2; ++u) {
        v4f D[4];
#pragma unroll
        for (int t = 0; t < 4; ++t) {
          D[t] = v4f{0, 0, 0, 0};
#pragma unroll
          for (int ks = 0; ks < 4; ++ks) {
            const v4u kf = *(const LAS v4u*)(L + L_K + (128 * a + 64 * u + 16 * (n & 3) + 4 * t + (n >> 2)) * 256 + (((4 * ks + g) ^ n) * 16));
            D[t] = mfma(kf, qa[ks], D[t]);
          }
        }
#pragma unroll
        for (int r = 0; r < 4; r += 2) {
          const u32 w = cvtpk(sel4(n >> 2, D[0][r], D[1][r], D[2][r], D[3][r]), sel4(n >> 2, D[0][r + 1], D[1][r + 1], D[2][r + 1], D[3][r + 1]));
          const u32 w2 = cvtpk(blo(w) * 0.08838834764831845f, bhi(w) * 0.08838834764831845f);
          sc[4 * u + r] = blo(w2); sc[4 * u + r + 1] = bhi(w2);
        }
      }
      float m = fmaxf(fmaxf(fmaxf(sc[0], sc[1]), fmaxf(sc[2], sc[3])), fmaxf(fmaxf(sc[4], sc[5]), fmaxf(sc[6], sc[7])));
      m = fmaxf(m, dppf<ROR4>(m)); m = fmaxf(m, dppf<ROR8>(m)); m = max32(max16(m));
      *(volatile LAS float*)(L + L_MX + (n & 3) * 16 + a * 4) = m;
      abar(3);
      { const v4f t = *(volatile LAS v4f*)(L + L_MX + (n & 3) * 16); m = fmaxf(fmaxf(t[0], t[1]), fmaxf(t[2], t[3])); }
      float e[8];
#pragma unroll
      for (int q = 0; q < 8; ++q) e[q] = ex2((sc[q] - m) * LOG2E);
      *(volatile LAS v4u*)(L + L_P + ((((n >> 2) * 4 + a) * 4 + g) * 4 + (n & 3)) * 16) = v4u{cvtpk(e[0], e[1]), cvtpk(e[2], e[3]), cvtpk(e[4], e[5]), cvtpk(e[6], e[7])};
      lflag(F_PDONE + 4 * a, 1);
      float l = ((e[0] + e[1]) + (e[2] + e[3])) + ((e[4] + e[5]) + (e[6] + e[7]));
      l += dppf<ROR4>(l); l += dppf<ROR8>(l); l = sum32(sum16(l));
      st8(rs, lane < 4 ? WS_ML + (grp * 4 + lane) * 256 + j * 32 + a * 8 : OOB, v2u{canon(f2u(m)), canon(f2u(l))});
    }
    TS(6);
    if (LPRIO != APRIO) __builtin_amdgcn_s_setprio(LPRIO);
    if (isW) {
      if (!NE || isQ || isL) {
        v2u r0[8], r1[8];
        if (WV) lwait4(F_KDONE, 3);
        load_w(r0, 0); load_w(r1, 1); tr4(r0, Bw[0]); SCHED(); load_w(r0, 2); tr4(r1, Bw[1]); SCHED(); load_w(r1, 3); tr4(r0, Bw[2]); SCHED(); tr4(r1, Bw[3]); SCHED();
      }
      if (LPRIO != APRIO) __builtin_amdgcn_s_setprio(APRIO);
      TS(8);
      float fr[4];
      {
        v2u t;
        SPIN(GSPIN) {
          t = ld8(rs, WS_ML + ((x * 4 + 2 * bp + (lane >> 5)) * 4 + a) * 256 + (lane & 31) * 8);
          if (!__any(umax(t.x, t.y) == SENT)) break;
          __builtin_amdgcn_s_sleep(2);
        }
        const float m = u2f(t.x);
        float M = fmaxf(m, dppf<ROR4>(m)); M = fmaxf(M, dppf<ROR8>(M)); M = max16(M);
        const float w = ex2((m - M) * LOG2E);
        float l = u2f(t.y) * w;
        l += dppf<XOR1>(l); l += dppf<XOR2>(l); l += dppf<ROR4>(l); l += dppf<ROR8>(l); l = sum16(l);
        const float f = w / l;
        fr[0] = dppf<NBC + 0>(f); fr[1] = dppf<NBC + 4>(f); fr[2] = dppf<NBC + 8>(f); fr[3] = dppf<NBC + 12>(f);
        PIN(fr[0]); PIN(fr[1]); PIN(fr[2]); PIN(fr[3]);
      }
      wgbar();
      TS(9);
      if (D3) __builtin_amdgcn_s_sleep(D3);
      v4u oa[4];
      const int ooff = WS_OP + ((x * 2 + bp) * 4 + a) * 4096 + n * 64 + g * 16, orot = 2048 * bp;
      SPIN(GSPIN) {
#pragma unroll
        for (int sl = 0; sl < 4; ++sl) oa[sl] = ld16(rs, ooff + ((sl * 1024) ^ orot));
        if (!__any(umax(umax(max4(oa[0]), max4(oa[1])), umax(max4(oa[2]), max4(oa[3]))) == SENT)) break;
      }
      TS(10);
      float t[4];
#pragma unroll
      for (int cc = 0; cc < 4; ++cc) {
        v4f ac = v4f{0, 0, 0, 0};
#pragma unroll
        for (int sl = 0; sl < 4; ++sl) ac = mfma(oa[sl], Bw[sl][cc], ac);
        t[cc] = (ac[0] * fr[0] + ac[1] * fr[1]) + (ac[2] * fr[2] + ac[3] * fr[3]);
      }
      swap16(t[0], t[2]); swap16(t[1], t[3]);
      *(LAS v2f*)(L + L_YP + a * 512 + (g >> 1) * 256 + n * 16 + (g & 1) * 8) = v2f{t[0] + t[2], t[1] + t[3]};
      wgbar();
      const v2f y0 = *(LAS v2f*)(L + L_YP + lane * 8), y1 = *(LAS v2f*)(L + L_YP + 512 + lane * 8), y2 = *(LAS v2f*)(L + L_YP + 1024 + lane * 8), y3 = *(LAS v2f*)(L + L_YP + 1536 + lane * 8);
      const u32 yw = canon(cvtpk((y0[0] + y1[0]) + (y2[0] + y3[0]), (y0[1] + y1[1]) + (y2[1] + y3[1])));
      const int yo = WS_Y + ((2 * a * 4 + 2 * bp + (lane >> 5)) * 8 + x) * 1024 + r8 * 128 + (lane & 31) * 4;
      st4(rs, yo, yw); st4(rs, yo + 32768, yw);
      TS(11);
    } else if (isF) {
      {
        v2u r0[8], r1[8]; v4u rd[8];
        const int mo = WS_MODL + (x * 4 + 2 * bp + (n >> 3)) * 8192 + fcol * 4;
        auto ldm = [&]() { mga = ld16(rs, mo); msb = ld16(rs, mo + 2048); mcb = ld16(rs, mo + 4096); mgb = ld16(rs, mo + 6144); };
        ldm();
        if (FE < 2) { load_w(r0, 0); load_w(r1, 1); tr4(r0, Bw[0]); SCHED(); load_w(r0, 2); tr4(r1, Bw[1]); SCHED(); load_w(r1, 3); }
        else if (FE < 4) { load_w(r0, 2); load_w(r1, 3); }
        if (FE < 4) { tr4(r0, Bw[2]); SCHED(); }
        if (!DE) load_d(rd);
        if (FE < 4) { tr4(r1, Bw[3]); SCHED(); }
        SPIN(GSPIN) {
          const u32 bad = ((mga.y ^ tag) | (mga.w ^ tag) | (msb.y ^ tag) | (msb.w ^ tag)) | ((mcb.y ^ tag) | (mcb.w ^ tag) | (mgb.y ^ tag) | (mgb.w ^ tag));
          if (!__any(bad != 0)) break;
          ldm();
        }
        TS(7);
        const u32 c01 = cvtpk(1.f + blo(mcb.x), 1.f + bhi(mcb.x)), c23 = cvtpk(1.f + blo(mcb.z), 1.f + bhi(mcb.z));
        mcb.x = c01; mcb.z = c23;
        const u32 h0 = dppu<SHL + 4>(c01), h1 = dppu<SHL + 4>(c23), l0 = dppu<0x110 + 4>(msb.x), l1 = dppu<0x110 + 4>(msb.z);
        const bool hi = n & 4;
        const v4u A = {hi ? l0 : c01, hi ? l1 : c23, hi ? msb.x : h0, hi ? msb.z : h1};
        v4f p;
#pragma unroll
        for (int cc = 0; cc < 4; ++cc) {
          v4f d[4];
#pragma unroll
          for (int sl = 0; sl < 4; ++sl) d[sl] = mfma(A, Bw[sl][cc], v4f{0, 0, 0, 0});
          p[cc] = (d[0][0] + d[1][1]) + (d[2][2] + d[3][3]);
        }
        *(volatile LAS v4f*)(L + L_PC + a * 1024 + g * 256 + n * 16) = p;
        if (!DE) {
          tr8(rd, Bd);
        }
      }
      wgbar();
      if (LPRIO != APRIO) __builtin_amdgcn_s_setprio(APRIO);
      const int ho = (n >> 3) * 512 + (8 * g + (n & 7)) * 4;
      float Cg, Cu, Bg, Bu;
      {
        const LAS float* pc = (const LAS float*)(L + L_PC + ho);
        Cg = (pc[0] + pc[256]) + (pc[512] + pc[768]); Cu = (pc[32] + pc[288]) + (pc[544] + pc[800]);
        Bg = (pc[64] + pc[320]) + (pc[576] + pc[832]); Bu = (pc[96] + pc[352]) + (pc[608] + pc[864]);
      }
      float xr[4] = {blo(xf.x), bhi(xf.x), blo(xf.y), bhi(xf.y)}, ga[4] = {blo(mga.x), bhi(mga.x), blo(mga.z), bhi(mga.z)};
      float c1[4] = {blo(mcb.x), bhi(mcb.x), blo(mcb.z), bhi(mcb.z)}, xc[4], gc[4];
#pragma unroll
      for (int r = 0; r < 4; ++r) { xc[r] = xr[r] * c1[r]; gc[r] = ga[r] * c1[r]; PIN(xc[r]); PIN(gc[r]); PIN(xr[r]); PIN(ga[r]); }
      v4u S0, S1;
      {
        const u32 one = (n & 1) ? 0x3F800000u : 0x3F80u;
#pragma unroll
        for (int m = 0; m < 4; ++m) { S0[m] = (n >> 1) == m ? one : 0u; S1[m] = (n >> 1) == m + 4 ? one : 0u; }
      }
      PIN(Cg); PIN(Cu); PIN(Bg); PIN(Bu); PIN(S0); PIN(S1);
      TS(8);
      if (D4) __builtin_amdgcn_s_sleep(D4);
      v4u y[4];
      const int yoff = WS_Y + ((x * 4 + 2 * bp + (n >> 3)) * 8 + g) * 1024 + a * 256 + (nn & 7) * 16;
      SPIN(GSPIN) {
        y[0] = ld16(rs, yoff); y[1] = ld16(rs, yoff + 4096); y[2] = ld16(rs, yoff + 128); y[3] = ld16(rs, yoff + 4096 + 128);
        if (!__any(umax(umax(max4(y[0]), max4(y[1])), umax(max4(y[2]), max4(y[3]))) == SENT)) break;
      }
      TS(12);
      v4f ys = mfma(S0, y[0], v4f{0, 0, 0, 0});
      ys = mfma(S0, y[1], ys); ys = mfma(S1, y[2], ys); ys = mfma(S1, y[3], ys);
      const u32 h01 = cvtpk(ys[0] * gc[0] + xc[0], ys[1] * gc[1] + xc[1]), h23 = cvtpk(ys[2] * gc[2] + xc[2], ys[3] * gc[3] + xc[3]);
      const v4u A = {h01, h23, dppu<SHL + 4>(h01), dppu<SHL + 4>(h23)};
      float x1[4];
#pragma unroll
      for (int r = 0; r < 4; ++r) x1[r] = ys[r] * ga[r] + xr[r];
      v4f pt;
#pragma unroll
      for (int cc = 0; cc < 4; ++cc) {
        v4f d[4];
#pragma unroll
        for (int sl = 0; sl < 4; ++sl) d[sl] = mfma(A, Bw[sl][cc], v4f{0, 0, 0, 0});
        pt[cc] = (d[0][0] + d[1][1]) + (d[2][2] + d[3][3]);
      }
      float s1 = (x1[0] + x1[1]) + (x1[2] + x1[3]), s2 = (x1[0] * x1[0] + x1[1] * x1[1]) + (x1[2] * x1[2] + x1[3] * x1[3]);
      swap32(s1, s2);
      float sv = sum16(s1 + s2);
      sv += dppf<XOR1>(sv); sv += dppf<XOR2>(sv); sv += dppf<0x141>(sv);
      *(LAS float*)(L + L_ST + (n >> 3) * 32 + (lane >> 5) * 16 + a * 4) = sv;
      *(LAS v4f*)(L + L_GP + a * 1024 + g * 256 + n * 16) = pt;
      wgbar();
      const LAS float* gp = (const LAS float*)(L + L_GP + ho);
      const float sg = (gp[0] + gp[256]) + (gp[512] + gp[768]), su = (gp[32] + gp[288]) + (gp[544] + gp[800]);
      const v4f q0 = *(LAS v4f*)(L + L_ST + (n >> 3) * 32), q1 = *(LAS v4f*)(L + L_ST + (n >> 3) * 32 + 16);
      const float mu = sum4(q0) * (1.f / 512.f), e2 = sum4(q1) * (1.f / 512.f);
      const float rstd = __builtin_amdgcn_rsqf(fmaxf(e2 - mu * mu, 0.f) + 1e-5f);
      const float act = silu((sg - mu * Cg) * rstd + Bg) * ((su - mu * Cu) * rstd + Bu);
      const u32 ap = cvtpk(act, dppf<SHL + 1>(act));
      const v4u A2 = {ap, dppu<SHL + 2>(ap), dppu<SHL + 4>(ap), dppu<SHL + 6>(ap)};
      float z[8];
#pragma unroll
      for (int cc = 0; cc < 8; ++cc) z[cc] = mfma(A2, Bd[cc], v4f{0, 0, 0, 0})[0];
      st16(rs, (g & 1) ? OOB : WS_Z + ((2 * bp + (g >> 1)) * 56 + fs) * 1024 + a * 256 + n * 16,
           v4u{canon(cvtpk(z[0], z[1])), canon(cvtpk(z[2], z[3])), canon(cvtpk(z[4], z[5])), canon(cvtpk(z[6], z[7]))});
      TS(13);
      const int id = 2 * x + r8;
      if (r8 < 2 && a == ((id & 7) >> 1)) {
        const int hf = id & 1, bt = 2 * bp + (id >> 3);
        const v4u S = hf ? S1 : S0;
        __builtin_amdgcn_s_sleep(D5);
        v4u zz[7];
        const int zoff = WS_Z + (bt * 56 + (n >> 3) * 4 + g) * 1024 + (id & 7) * 128 + (nn & 7) * 16;
        SPIN(GSPIN) {
          u32 mx = 0;
#pragma unroll
          for (int i = 0; i < 7; ++i) zz[i] = ld16(rs, zoff + i * 8192);
#pragma unroll
          for (int i = 0; i < 7; ++i) mx = umax(mx, max4(zz[i]));
          if (!__any(mx == SENT)) break;
        }
        TS(14);
        v4f e0 = mfma(S, zz[0], v4f{0, 0, 0, 0}), e1 = mfma(S, zz[1], v4f{0, 0, 0, 0});
        e0 = mfma(S, zz[2], e0); e1 = mfma(S, zz[3], e1); e0 = mfma(S, zz[4], e0); e1 = mfma(S, zz[5], e1); e0 = mfma(S, zz[6], e0);
        float o[4];
        const float gb[4] = {blo(mgb.x), bhi(mgb.x), blo(mgb.z), bhi(mgb.z)};
#pragma unroll
        for (int r = 0; r < 4; ++r) { float v = e0[r] + e1[r]; v += dppf<ROR8>(v); o[r] = x1[r] + gb[r] * v; }
        if ((n >> 3) == (id >> 3) && (g >> 1) == hf) *(v2u*)(out + bt * 512 + fcol) = v2u{cvtpk(o[0], o[1]), cvtpk(o[2], o[3])};
      }
    }
  }
  TS(15);
}

extern "C" void kernel_launch(void* const* d_in, const int* in_sizes, int n_in, void* d_out, int out_size, void* d_ws, size_t ws_size,
                              hipStream_t stream) {
  fused_kernel<<<256, 512, 0, stream>>>((const u16*)d_in[0], (const u16*)d_in[1], (const u16*)d_in[2], (const u16*)d_in[3], (const u16*)d_in[4],
                                        (const u16*)d_in[6], (const u16*)d_in[7], (const u16*)d_in[8], (const u16*)d_in[9], (const u16*)d_in[10],
                                        (u16*)d_out, (unsigned char*)d_ws);
}
```

```cpp
#include <hip/hip_runtime.h>
#include <hip/hip_bf16.h>
#include <stdint.h>

#if defined(__HIP_DEVICE_COMPILE__) && !defined(PK)
#pragma clang attribute push(__attribute__((target("no-packed-fp32-ops"))), apply_to = function)
#endif
typedef unsigned u32;
typedef unsigned short u16;
typedef unsigned long long u64;
typedef u32 v4u __attribute__((ext_vector_type(4)));
typedef u32 v2u __attribute__((ext_vector_type(2)));
typedef float v4f __attribute__((ext_vector_type(4)));
typedef float v8f __attribute__((ext_vector_type(8)));
typedef u32 v32u __attribute__((ext_vector_type(32)));
typedef float v2f __attribute__((ext_vector_type(2)));
typedef __bf16 v2b __attribute__((ext_vector_type(2)));
typedef short v8s __attribute__((ext_vector_type(8)));
#define LAS __attribute__((address_space(3)))
#define DEV __device__ __forceinline__

#define WH 8
#define WM 4
#define WQ 8
#define WW 8
#define PAM 1
#define PAQ 8
#define N1 0
#define W1 8
#define N2 4
#define W2 4
#define PWQ 0
#define PWM 0
#define PV 200
#define PVF 180
#define WE 2
#define TW2 9200
#define PW2 180
#define TF2 9500
#define PF2 0
#define TW1 0
#define SL 800
#define I1 33
#define I2 64
#define PE 120
#define PEF PE
#define TP 6800
#define DE 0
#define TY 0
#define TZ 0
#define TWG 0
#define DM 2
#define WARM 0
#define TWA 0
#define TWB 0
#define TWC 0
#define OAUX 0
#define KB 2
#define FE 3
#define RS 1
#define RL 1
#define RY 4
#define D0 2
#define D1 4
#define D3 1
#define D4 1
#define D5 8
#define VQ 8
#define VM 8
#define VW 8
#define VSW 33
#define FLATE 1
#define PRE 2
#define PB 7
#define TQ 3800
#define PWO POFF
#define PWF 220
#define PWW 300
#define PWL 400
#define PACEF 180
#define PACE 190
#define POFF 200
#define CPF 1
#define CPS 80
#define XW 4
#define PEEL 1
#define F3K 4
#define DK 5
#define TML 0
#define QE 3
#define TPF TP
#define SEP 1
#define RB 2
#define KS 1
#define FM 1
#define FX 0
#define MIX 1
#define KQ 1
#define PACEE PACE
#define PVE PV
#define PACEG PACEF
#define PVG PVF
#define EL 0
#define TEL 0
#define NOL 0
#define EK 4
#define BI EK
#define EH 1
#define MLE 1
#define APRIO 3

constexpr int WS_EPOCH = 0;
constexpr int WS_SAT = 4096;
constexpr int WS_Q = WS_SAT + 131072;
constexpr int WS_MODL = WS_Q + 131072;
constexpr int WS_ML = WS_MODL + 131072;
constexpr int WS_OP = WS_ML + 8192;
constexpr int WS_Y = WS_OP + 262144;
constexpr int WS_Z = WS_Y + 262144;
constexpr int WS_END = WS_Z + 262144;
constexpr int WS_STAMP = 4 << 20;
constexpr int OOB = 0x7FFFFF00;

constexpr int L_K = 0;
constexpr int L_P = 131072;
constexpr int L_OPW = L_P + 4096;
constexpr int L_FL = L_OPW + 8192;
constexpr int F_KDONE = L_FL, F_PDONE = L_FL + 16, F_PUB = L_FL + 32, F_ABAR = L_FL + 48, F_TAG = L_FL + 64;
constexpr int L_DP = L_FL + 256;
constexpr int L_Q = L_DP + 4096;
constexpr int L_MX = L_Q + 1088;
constexpr int L_PC = L_MX + 128;
constexpr int L_ST = L_PC + 8192;
constexpr int L_JUNK = L_ST + 128;
constexpr int L_TOTAL = L_JUNK + 1024;

constexpr u32 SENT = 0xFFFFFFFFu, LSB = 0x00010001u;
constexpr int GSPIN = 1 << 16, LSPIN = 1 << 20;

DEV u64 bal(bool p) { return __builtin_amdgcn_ballot_w64(p); }
DEV bool any(bool p) { return bal(p) != 0; }
DEV float u2f(u32 u) { return __builtin_bit_cast(float, u); }
DEV u32 f2u(float f) { return __builtin_bit_cast(u32, f); }
DEV u32 cvtpk(float a, float b) { v2f f = {a, b}; return __builtin_bit_cast(u32, __builtin_convertvector(f, v2b)); }
DEV float blo(u32 p) { return u2f(p << 16); }
DEV float bhi(u32 p) { return u2f(p & 0xFFFF0000u); }
DEV float rb(float f) { return blo(cvtpk(f, f)); }
DEV u32 canon(u32 u) { return u < 0xFFFFFFFEu ? u : 0xFFFFFFFEu; }
DEV float ex2(float v) { return __builtin_amdgcn_exp2f(v); }
DEV float rcp(float v) { return __builtin_amdgcn_rcpf(v); }
constexpr float LOG2E = 1.4426950408889634f;
DEV float silu(float v) { return v * rcp(1.f + ex2(-LOG2E * v)); }
template <int C> DEV u32 dppu(u32 v) { return (u32)__builtin_amdgcn_mov_dpp((int)v, C, 0xf, 0xf, true); }
template <int C, int B> DEV u32 upd(u32 old, u32 v) { return (u32)__builtin_amdgcn_update_dpp((int)old, (int)v, C, 0xf, B, false); }
template <int C> DEV float dppf(float v) { return u2f(dppu<C>(f2u(v))); }
constexpr int XOR1 = 0xB1, XOR2 = 0x4E, ROR4 = 0x124, ROR8 = 0x128, SHL = 0x100, NBC = 0x150;
DEV void swap32(float& a, float& b) { auto r = __builtin_amdgcn_permlane32_swap(f2u(a), f2u(b), false, false); a = u2f(r[0]); b = u2f(r[1]); }
DEV void swap32u(u32& a, u32& b) { auto r = __builtin_amdgcn_permlane32_swap(a, b, false, false); a = r[0]; b = r[1]; }
DEV void swap16(float& a, float& b) { auto r = __builtin_amdgcn_permlane16_swap(f2u(a), f2u(b), false, false); a = u2f(r[0]); b = u2f(r[1]); }
DEV float sum16(float v) { float a = v, b = v; swap16(a, b); return a + b; }
DEV float sum32(float v) { float a = v, b = v; swap32(a, b); return a + b; }
DEV float max16(float v) { float a = v, b = v; swap16(a, b); return fmaxf(a, b); }
DEV float max32(float v) { float a = v, b = v; swap32(a, b); return fmaxf(a, b); }
DEV float wsum(float v) { v += dppf<XOR1>(v); v += dppf<XOR2>(v); v += dppf<ROR4>(v); v += dppf<ROR8>(v); return sum32(sum16(v)); }
DEV v4f mfma(v4u a, v4u b, v4f c) { return __builtin_amdgcn_mfma_f32_16x16x32_bf16(__builtin_bit_cast(v8s, a), __builtin_bit_cast(v8s, b), c, 0, 0, 0); }
DEV u32 plo(u32 k0, u32 k1) { return __builtin_amdgcn_perm(k1, k0, 0x05040100u); }
DEV u32 phi(u32 k0, u32 k1) { return __builtin_amdgcn_perm(k1, k0, 0x07060302u); }
DEV float sel4(int g, float a, float b, float c, float d) { float lo = (g & 1) ? b : a, hi = (g & 1) ? d : c; return (g & 2) ? hi : lo; }
DEV u32 umax(u32 a, u32 b) { return a > b ? a : b; }
DEV u32 max4(v4u v) { return umax(umax(v.x, v.y), umax(v.z, v.w)); }
DEV float sum4(v4f v) { return (v[0] + v[1]) + (v[2] + v[3]); }
DEV v4u tw2(u32 tag, int byte) { const u32 t = tag >> (8 * byte); return v4u{t & 3u, (t >> 2) & 3u, (t >> 4) & 3u, (t >> 6) & 3u}; }
DEV v4u tag4(v4f p, v4u t) { return v4u{((f2u(p[0]) + 2u) & ~3u) | t.x, ((f2u(p[1]) + 2u) & ~3u) | t.y, ((f2u(p[2]) + 2u) & ~3u) | t.z, ((f2u(p[3]) + 2u) & ~3u) | t.w}; }
DEV float untag(u32 v) { return u2f(v & 0xFFFFFF00u); }
DEV u32 tb(u32 t) { return (t & 1u) | ((t & 2u) << 15); }
DEV v4u tw4(u32 tag, int byte) { const u32 t = tag >> (8 * byte); return v4u{tb(t), tb(t >> 2), tb(t >> 4), tb(t >> 6)}; }
DEV u32 pkt(float a, float b, u32 t) { return ((__builtin_amdgcn_perm(f2u(b), f2u(a), 0x07060302u) + LSB) & ~LSB) | t; }
#define LIKELY(c) __builtin_expect(!!(c), 1)
#define UNLIKELY(c) __builtin_expect(!!(c), 0)
DEV u32 or4(v4u v) { return (v.x | v.y) | (v.z | v.w); }
#define CBAR() asm volatile("" ::: "memory")
#define SCHED() __builtin_amdgcn_sched_barrier(0)
#define SGB(m, c) __builtin_amdgcn_sched_group_barrier(m, c, 0)
#define PIN(v) asm volatile("" : "+v"(v))
#define KEEP(v) asm volatile("s_waitcnt vmcnt(1)" :: "v"(v))
template <int N> struct IC { static constexpr int value = N; };
#define SPIN(N) _Pragma("unroll 1") for (int sp = 0; sp < (N); ++sp)

typedef __amdgpu_buffer_rsrc_t rsrc_t;
DEV v4u ld16(rsrc_t rs, int off, int so = 0) { CBAR(); v4u v = __builtin_amdgcn_raw_buffer_load_b128(rs, off, so, 16); CBAR(); return v; }
DEV v2u ld8(rsrc_t rs, int off) { CBAR(); v2u v = __builtin_amdgcn_raw_buffer_load_b64(rs, off, 0, 16); CBAR(); return v; }
DEV u32 ld4(rsrc_t rs, int off) { CBAR(); u32 v = __builtin_amdgcn_raw_buffer_load_b32(rs, off, 0, 16); CBAR(); return v; }
DEV void st16(rsrc_t rs, int off, v4u v) { CBAR(); __builtin_amdgcn_raw_buffer_store_b128(v, rs, off, 0, 16); CBAR(); }
DEV void st8(rsrc_t rs, int off, v2u v) { CBAR(); __builtin_amdgcn_raw_buffer_store_b64(v, rs, off, 0, 16); CBAR(); }
DEV void st4(rsrc_t rs, int off, u32 v, int so = 0) { CBAR(); __builtin_amdgcn_raw_buffer_store_b32(v, rs, off, so, 16); CBAR(); }

DEV void dma16(const void* sbase, u32 voff, u32 ldsdst) {
  u32 keep;
  asm volatile("s_mov_b32 %0, m0\n\ts_mov_b32 m0, %3\n\ts_nop 0\n\tglobal_load_lds_dwordx4 %1, %2 nt\n\ts_mov_b32 m0, %0" : "=&s"(keep) : "v"(voff), "s"(sbase), "s"(ldsdst) : "memory");
}
DEV void ald16(v4u& d, v4u rd, int voff, int soff) {
  asm volatile("buffer_load_dwordx4 %0, %1, %2, %3 offen sc1" : "=v"(d) : "v"(voff), "s"(rd), "s"(soff) : "memory");
}
template <int N> DEV void vmwait() { asm volatile("s_waitcnt vmcnt(%0)" ::"i"(N) : "memory"); }
DEV void wgbar() { asm volatile("s_waitcnt lgkmcnt(0)\n\ts_barrier" ::: "memory"); }

#define TS(slot) do {} while (0)

#define CT(i) do {} while (0)
#define CTOUT(n) do {} while (0)

DEV void tr4(v2u* r, v4u* B, bool mix = false) {
  if (mix) {
#pragma unroll
    for (int i = 0; i < 8; ++i) {
      const u32 y = (u32)__builtin_amdgcn_update_dpp((int)r[i].y, (int)r[i].x, ROR8, 0xf, 0x3, false);
      r[i].x = (u32)__builtin_amdgcn_update_dpp((int)r[i].x, (int)r[i].y, ROR8, 0xf, 0xc, false);
      r[i].y = y;
    }
  }
#pragma unroll
  for (int m = 0; m < 4; ++m) {
    B[0][m] = plo(r[2 * m].x, r[2 * m + 1].x); B[1][m] = phi(r[2 * m].x, r[2 * m + 1].x);
    B[2][m] = plo(r[2 * m].y, r[2 * m + 1].y); B[3][m] = phi(r[2 * m].y, r[2 * m + 1].y);
  }
  PIN(B[0]); PIN(B[1]); PIN(B[2]); PIN(B[3]);
}

__global__ void __launch_bounds__(512, 2)
fused_kernel(const u16* __restrict__ xin, const u16* __restrict__ cin, const u16* __restrict__ wq, const u16* __restrict__ wmod,
             const u16* __restrict__ kc, const u16* __restrict__ vc, unsigned char* ws, const u16* __restrict__ wo,
             const u16* __restrict__ wg, const u16* __restrict__ wu, const u16* __restrict__ wd, u16* __restrict__ out) {
  __shared__ __attribute__((aligned(16))) unsigned char lds_[L_TOTAL];
  LAS unsigned char* L = (LAS unsigned char*)lds_;
  const int tid = threadIdx.x, lane = tid & 63, wave = __builtin_amdgcn_readfirstlane(tid >> 6);
  const int bid = blockIdx.x, x = bid & 7, idx = bid >> 3, b = idx >> 3, j = idx & 7;
  const int n = lane & 15, g = lane >> 4;
  const bool isM = idx < 4, isL = MIX ? idx >= 8 && idx < 12 : idx >= 4 && idx < 8, isQ = MIX ? b < 2 && j >= 4 : b == 1;
  const int jq = MIX ? 4 * b + (j & 3) : j;
  const int xs = SEP == 1 ? x & 1 : x >> 2, xh = SEP == 1 ? x >> 1 : x & 3;
  const bool isW = SEP ? b >= 2 && !xs : b == 2, isF = SEP ? b >= 2 && xs && idx < 30 : b == 3 && (j < 6 || !(x & 1));
  const bool first = isM || isL || isQ;
#define rs __builtin_amdgcn_make_buffer_rsrc(ws, 0, WS_END, 0x00020000)
#define rd4 (v4u{(u32)(uintptr_t)ws, (u32)((uintptr_t)ws >> 32) & 0xFFFFu, (u32)WS_END, 0x00020000u})
  auto lflag = [&](int off, u32 v) { *(volatile LAS u32*)(L + off) = v; };
  auto lwait4 = [&](int off, u32 v) {
    SPIN(LSPIN) { v4u f = *(volatile LAS v4u*)(L + off); if (min(min(f.x, f.y), min(f.z, f.w)) >= v) break; __builtin_amdgcn_s_sleep(1); }
  };
  auto now = [&]() { CBAR(); const u32 t = (u32)__builtin_amdgcn_s_memrealtime(); CBAR(); return t; };
  const u32 junk = (u32)(uintptr_t)L + L_JUNK;
  CBAR();
  const u64 t64 = __builtin_amdgcn_s_memrealtime();
  CBAR();
  const u32 t0 = (u32)t64;
  if (CPF) {
    u64 pc; u32 len;
    asm volatile("s_getpc_b64 %0\n.Lfk_begin%=:\n\ts_mov_b32 %1, .Lfk_end - .Lfk_begin%=" : "=s"(pc), "=s"(len));
    const u32 o = min((u32)(idx * CPS + tid) * 16u, len - 16u);
    if (tid < CPS) asm volatile("s_mov_b32 m0, %2\n\ts_nop 0\n\tglobal_load_lds_dwordx4 %0, %1" :: "v"(o), "s"(pc), "s"(junk) : "memory");
  }
  const unsigned char* kcb = (const unsigned char*)kc;
  const bool urgent = (!PWM && isM) || (!PWQ && isQ);
#define KVADDR(W) \
  const int s = (W); \
  const bool kq = KQ && b < 2; \
  const bool ks = KS == 2 || (KS && b == 3 && xs) || kq; \
  const u32 rh = ((u32)((kq ? 0x1003020013121101ull : 0x1101131210030200ull) >> (8 * j)) & 255u) << 8; \
  const u32 rh1 = kq ? ((u32)(0x1003020011011312ull >> (8 * j)) & 255u) << 8 : rh; \
  const size_t kvoff = ((size_t)(b * 8 + x) << 20) + (size_t)s * (ks ? 8192 : 1024); \
  auto co = [&](int i) { return (size_t)i * 32768 + (size_t)(KS != 2 && ks ? ((i & 1) ? rh1 : rh) : KS == 2 ? ((j + i) & 3) * 256 + ((j + i) & 4) * 1024 : ((j + i) & 7) * 4096); }; \
  const int gs = g * (ks ? 1024 : 256); \
  const unsigned char* kp = kcb + kvoff; \
  const unsigned char* vp = (const unsigned char*)vc + kvoff + gs + n * 16; \
  const u32 lk = (u32)(uintptr_t)L + L_K + s * 1024;
#define ARGS() asm volatile("" :: "s"(xin), "s"(cin), "s"(wq), "s"(wmod), "s"(kc), "s"(vc), "s"(ws), "s"(wo), "s"(wg), "s"(wu), "s"(t64))
  int w0 = wave;
  asm volatile("" : "+s"(w0));
  if (LIKELY(EK && w0 < 4 && !urgent)) {
    KVADDR(w0)
    u32 ev[EK ? EK : 1];
#pragma unroll
    for (int i = 0; i < EK; ++i) { ev[i] = (u32)(kvoff + co(i)) + gs + ((n ^ ((i & 3) | (g << 2))) * 16); PIN(ev[i]); }
    ARGS();
#pragma unroll
    for (int i = 0; i < EK; ++i) dma16(kcb, ev[i], lk + i * 4096);
  }
  TS(0);

  if (wave < 4) {
    ARGS();
    KVADDR(wave)
    const int wsel = isF ? 0 : isQ ? 1 : isM ? 2 : 3;
    auto win = [&](int i) {
      u32 t = now();
      const int k = (i > I1 ? I1 : i > PB ? i : PB) - PB, kv = (i > I2 ? I2 : i > I1 ? i : I1) - I1, ke = i > I2 ? i - I2 : 0;
      if (i <= PAQ + N1) { if (wsel == 1) vmwait<W1 - 1>(); }
      else if (i <= PAQ + N1 + N2) { if (wsel == 1) vmwait<W2 - 1>(); }
      if (i <= I2 && t - t0 < (k * PACE + kv * PV + POFF + i * SL / 64) / 10) {
        if ((i < 33 ? WM : VM) != WH && wsel == 2) { if (i < 33) vmwait<WM>(); else vmwait<VM>(); }
        if ((i < 33 ? WW : VW) != WH && wsel == 3) { if (i < 33) vmwait<WW>(); else vmwait<VW>(); }
      }
      vmwait<WH>();
      if (PACE && i >= PRE) {
        const u32 dl = j < 4 ? (wsel ? (k * PACEE + kv * PVE + ke * PVE + POFF) / 10 : (k * PACEG + kv * PVG + ke * PVG + POFF) / 10)
                             : (wsel ? (k * PACE + kv * PV + ke * PE + POFF) / 10 : (k * PACEF + kv * PVF + ke * PVF + POFF) / 10);
        while (t - t0 < dl) t = now();
      }
    };
    v4u va[32];
    v4u T[4][8];
    if (urgent) __builtin_amdgcn_s_barrier();
    if (!EK) win(0);
    va[0] = __builtin_nontemporal_load((const v4u*)(vp + co(0)));
    SCHED();
    TS(29);
    if (!EK) win(1);
#pragma unroll
    for (int i = 0; i < 32; ++i) {
      if (!PWQ && i == PAQ && N1 && isQ) lwait4(F_PUB, 1);
      if (!PWQ && i == PAQ + N1 && N2 && isQ) lwait4(F_PUB, 2);
      if ((!PWM && i == PAM && isM) || (!PWQ && i == PAQ + N1 + N2 && isQ)) lwait4(F_PUB, 3);
      if (i >= EK || urgent) dma16(kp + co(i), gs + ((n ^ ((i & 3) | (g << 2))) * 16), lk + i * 4096);
      if (i == BI && !urgent) __builtin_amdgcn_s_barrier();
      if (i + 1 >= EK || urgent) win(i + 2);
    }
    lflag(F_KDONE + 4 * s, 2);
    TS(1);
#pragma unroll
    for (int i = 1; i < 32; ++i) {
      va[i] = __builtin_nontemporal_load((const v4u*)(vp + co(i)));
      SCHED();
      if (i == 31) break;
      win(i + 33);
      if (i == WH) lflag(F_KDONE + 4 * s, 3);
      const int c = i - WH;
      if (c >= 0 && (c & 1)) {
        const int G = c >> 3, m = (c & 7) >> 1;
#pragma unroll
        for (int q = 0; q < 4; ++q) { u32 t0 = plo(va[c - 1][q], va[c][q]), t1 = phi(va[c - 1][q], va[c][q]); PIN(t0); PIN(t1); T[G][2 * q][m] = t0; T[G][2 * q + 1][m] = t1; }
      }
      SCHED();
    }
    lflag(F_KDONE + 4 * s, 4);
    TS(2);
    lwait4(F_PDONE, 1);
    TS(3);
    v4u pa[4];
#pragma unroll
    for (int G = 0; G < 4; ++G) pa[G] = *(volatile LAS v4u*)(L + L_P + (((s * 4 + G) * 4 + g) * 4 + (n >> 2)) * 16);
    u32 tbits = tb(*(volatile LAS u32*)(L + F_TAG) >> (2 * n));
    PIN(tbits);
    v4f acc[8];
#pragma unroll
    for (int d = 0; d < 8; ++d) acc[d] = v4f{0, 0, 0, 0};
    constexpr int CDONE = (31 - WH) & ~1;
#pragma unroll
    for (int G = 0; G < 4; ++G) {
#pragma unroll
      for (int m = 0; m < 4; ++m) {
        const int c = G * 8 + 2 * m;
        if (c >= CDONE) {
#pragma unroll
          for (int q = 0; q < 4; ++q) { T[G][2 * q][m] = plo(va[c][q], va[c + 1][q]); T[G][2 * q + 1][m] = phi(va[c][q], va[c + 1][q]); }
          if (c < 30) SCHED();
        }
      }
#pragma unroll
      for (int d = 0; d < 8; ++d) acc[d] = mfma(pa[G], T[G][d], acc[d]);
      if (G < 3) SCHED();
    }
    *(volatile LAS v4u*)(L + L_OPW + s * 2048 + lane * 32) = v4u{canon(f2u(acc[0][0])), canon(f2u(acc[1][0])), canon(f2u(acc[2][0])), canon(f2u(acc[3][0]))};
    *(volatile LAS v4u*)(L + L_OPW + s * 2048 + lane * 32 + 16) = v4u{canon(f2u(acc[4][0])), canon(f2u(acc[5][0])), canon(f2u(acc[6][0])), canon(f2u(acc[7][0]))};
    TS(4);
    {
      v2u o0, o1, o2, o3;
      SPIN(LSPIN) {
        o0 = *(volatile LAS v2u*)(L + L_OPW + 0 * 2048 + s * 512 + lane * 8); o1 = *(volatile LAS v2u*)(L + L_OPW + 1 * 2048 + s * 512 + lane * 8);
        o2 = *(volatile LAS v2u*)(L + L_OPW + 2 * 2048 + s * 512 + lane * 8); o3 = *(volatile LAS v2u*)(L + L_OPW + 3 * 2048 + s * 512 + lane * 8);
        u32 mx = umax(umax(umax(o0.x, o0.y), umax(o1.x, o1.y)), umax(umax(o2.x, o2.y), umax(o3.x, o3.y)));
        if (!any(mx == SENT)) break;
      }
      const float e = (u2f(o0.x) + u2f(o1.x)) + (u2f(o2.x) + u2f(o3.x)), o = (u2f(o0.y) + u2f(o1.y)) + (u2f(o2.y) + u2f(o3.y));
      st4(rs, WS_OP + ((x * 4 + s) * 4 + g) * 2048 + (j >> 2) * 1024 + (b * 4 + (j & 3)) * 64 + n * 4, pkt(e, o, tbits));
    }
    lflag(F_KDONE + 4 * s, 5);
    TS(5);
  } else {
    const int a = wave - 4;
#define ROLES \
    const int fo = ((0x3625140 >> (2 * (idx & 14))) & 7) + xh; \
    const int fs = FM ? 14 * xh + 2 * (fo >= 7 ? fo - 7 : fo) + (j & 1) : SEP ? 14 * xh + idx - 16 : 14 * (x >> 1) + 8 * (x & 1) + j; \
    const int xw = SEP == 1 ? (x + XW + (b & 1)) & 7 : SEP ? ((x + XW) & 3) + 4 * (b & 1) : (x + XW) & 7;
    constexpr int fp = 0;
    __builtin_amdgcn_s_setprio(APRIO);
    u32 ep;
    u32 tag;
    auto gettag = [&]() {
      PIN(ep);
      const u32 t = (((u32)__builtin_amdgcn_readfirstlane(ep) & 0x7F7F7F7Fu) + 0x01010101u) & 0x7F7F7F7Fu;
      tag = t | ((~t & 0x01010101u) << 7);
    };
    auto ldsinit = [&]() {
#pragma unroll
      for (int i = 0; i < 2; ++i) *(volatile LAS v4u*)(L + L_OPW + (i * 256 + a * 64 + lane) * 16) = v4u{SENT, SENT, SENT, SENT};
      if (a == 0) *(volatile LAS u32*)(L + L_FL + lane * 4) = 0;
      wgbar();
    };
    auto abar = [&](u32 ph) { lflag(F_ABAR + 4 * a, ph); lwait4(F_ABAR, ph); };

    v4u Bw[4][4], Bd[8];
    const int nn = n ^ (FX * fp);
    const unsigned char* wwp; int wws, wwi0, wwi1;
    u32 pw = PWL / 10;
    auto pace = [&](u32 dl) { while (now() - t0 < dl + PWO / 10) __builtin_amdgcn_s_sleep(1); };
    int pw2 = 0;
    auto load_w = [&](v2u* r, int sl, bool paced = true) {
      const unsigned char* p = wwp + (sl ^ ((FX & 3) * fp)) * wws;
#pragma unroll
      for (int i = 0; i < 8; ++i) {
        if (paced) pace(pw2 == 1 ? (TW2 - PWO + (8 * (sl - WE) + i) * PW2) / 10 : pw2 == 2 ? (TF2 - PWO + (8 * (sl - FE) + i) * PF2) / 10 : (8 * sl + i) * pw);
        r[i] = __builtin_nontemporal_load((const v2u*)(p + ((i >> 2) ^ ((FX >> 2) * fp)) * wwi1 + (i & 3) * wwi0));
        SCHED();
      }
    };
    auto tr8 = [&](const v4u* rd, v4u* B) {
#pragma unroll
      for (int m = 0; m < 4; ++m)
#pragma unroll
        for (int q = 0; q < 4; ++q) { B[2 * q][m] = plo(rd[2 * m][q], rd[2 * m + 1][q]); B[2 * q + 1][m] = phi(rd[2 * m][q], rd[2 * m + 1][q]); }
#pragma unroll
      for (int q = 0; q < 8; ++q) PIN(B[q]);
    };

    v4u qv;
#define QO (WS_Q + (x * 4 + b) * 4096 + a * 1024 + lane * 16)
    auto tpw = [&](int T) { while (now() - t0 < T / 10) __builtin_amdgcn_s_sleep(1); };
    if (LIKELY(first)) {
      const int mt = 4 * x + (j & 3);
      const int kh = j & 1;
      v2u c0, c1;
      v4u r[2][8];
      if (LIKELY(urgent)) {
        const int wst = isQ ? 8192 : 6144, row0 = 256 * kh + 64 * a;
        int cq = lane * 8 + a * 1024, cm_ = (n >> 2) * 1024 + 32 * (n & 1) + 8 * g + row0 * 2, sq = x * 1024 + 256 * (jq >> 1), sm = 128 * (mt >> 1);
        PIN(cq); PIN(cm_); asm volatile("" : "+s"(sq), "+s"(sm));
        int co_ = isQ ? cq : cm_, cs = isQ ? 512 : 64;
        int vo = (16 * (n >> 3) + 4 * g) * wst + 16 * (n & 7);
        int s0 = row0 * wst + (isQ ? sq : sm);
        PIN(co_); PIN(vo); asm volatile("" : "+s"(cs), "+s"(s0));
        ARGS();
        SCHED();
        const rsrc_t rc = __builtin_amdgcn_make_buffer_rsrc((void*)(isQ ? xin : cin), 0, 4096, 0x00020000);
        c0 = __builtin_amdgcn_raw_buffer_load_b64(rc, co_, 0, 0);
        c1 = __builtin_amdgcn_raw_buffer_load_b64(rc, co_, cs, 0);
        SCHED();
        const rsrc_t rw = __builtin_amdgcn_make_buffer_rsrc((void*)(isQ ? wq : wmod), 0, 512 * wst, 0x00020000);
#pragma unroll
        for (int i = 0; i < 8; ++i) { r[0][i] = __builtin_amdgcn_raw_buffer_load_b128(rw, vo, s0 + (32 * (i >> 2) + (i & 3)) * wst, 2); SCHED(); }
        if (isQ) {
#pragma unroll
          for (int i = 0; i < 8; ++i) { r[1][i] = __builtin_amdgcn_raw_buffer_load_b128(rw, vo + 128, s0 + (32 * (i >> 2) + (i & 3)) * wst, 2); SCHED(); }
        }
        ep = ld4(rs, WS_EPOCH);
      } else {
        ARGS();
        ep = ld4(rs, WS_EPOCH);
        const unsigned char* wp = (const unsigned char*)wmod + (size_t)(128 * a + 16 * (n >> 3) + 4 * g) * 6144 + (1024 + 64 * mt + 8 * (n & 7)) * 2;
        const u16* sp = cin + (n >> 2) * 512 + 128 * a + 16 * (n & 3) + 4 * g;
        c0 = *(const v2u*)sp; c1 = *(const v2u*)(sp + 64);
        SCHED();
        ldsinit();
#pragma unroll
        for (int i = 0; i < 16; ++i) { pace(i * pw); r[i >> 3][i & 7] = __builtin_nontemporal_load((const v4u*)(wp + (size_t)(32 * (i >> 3) + 64 * ((i >> 2) & 1) + (i & 3)) * 6144)); SCHED(); }
      }
      const int ak = 64 * a + 16 * (n & 1) + 4 * g;
      const int so = WS_SAT + a * 8192 + kh * 1024 + lane * 16;
      v4u sv[4];
      if (PWQ && isQ) {
#pragma unroll
        for (int i = 0; i < 4; ++i) sv[i] = ld16(rs, so + i * 2048);
      }
      TS(16);
      if (urgent) ldsinit();
      v4u A;
      if (isQ) {
        const float xs[8] = {blo(c0.x), bhi(c0.x), blo(c0.y), bhi(c0.y), blo(c1.x), bhi(c1.x), blo(c1.y), bhi(c1.y)};
        const float mean = wsum(((xs[0] + xs[1]) + (xs[2] + xs[3])) + ((xs[4] + xs[5]) + (xs[6] + xs[7]))) * (1.f / 512.f);
        float sq = 0;
#pragma unroll
        for (int q = 0; q < 8; ++q) sq += (xs[q] - mean) * (xs[q] - mean);
        const float var = wsum(sq) * (1.f / 512.f);
        const float mb = rb(mean), rs_ = rcp(rb(__builtin_amdgcn_sqrtf(rb(rb(var) + 1e-5f))));
        const v2u ch = kh ? c1 : c0;
#pragma unroll
        for (int q = 0; q < 2; ++q) { const u32 d = cvtpk(blo(ch[q]) - mb, bhi(ch[q]) - mb); A[q] = cvtpk(blo(d) * rs_, bhi(d) * rs_); }
        A[2] = A[3] = 0;
      } else {
        A = v4u{cvtpk(silu(blo(c0.x)), silu(bhi(c0.x))), cvtpk(silu(blo(c0.y)), silu(bhi(c0.y))), cvtpk(silu(blo(c1.x)), silu(bhi(c1.x))), cvtpk(silu(blo(c1.y)), silu(bhi(c1.y)))};
      }
      PIN(A);
      SCHED();
      v4u B0[8], B1[8];
      tr8(r[0], B0); SCHED();
      if (!isM) { tr8(r[1], B1); SCHED(); }
      gettag();
      const v4u t2 = tw2(tag, n & 3);
      TS(1);
      if (isQ) {
        lflag(F_PUB + 4 * a, 1);
        bool need = true;
        if (!PWQ && TQ) { while (now() - t0 < TQ / 10) __builtin_amdgcn_s_sleep(1); }
        SPIN(GSPIN) {
          if (need) {
            if (!PWQ || sp) {
#pragma unroll
              for (int i = 0; i < 4; ++i) sv[i] = ld16(rs, so + i * 2048);
            }
#pragma unroll
            for (int i = 0; i < 4; ++i) sv[i] ^= t2;
            u32 w = (or4(sv[0]) | or4(sv[1])) | (or4(sv[2]) | or4(sv[3]));
            w |= dppu<XOR1>(w); w |= dppu<XOR2>(w);
            need = (w & 3u) != 0;
          }
          if (!any(need)) break;
          if (D0) __builtin_amdgcn_s_sleep(D0);
        }
        lflag(F_PUB + 4 * a, 2);
        TS(2);
        v2u h;
#pragma unroll
        for (int q = 0; q < 2; ++q) {
          const u32 sa = cvtpk(u2f(sv[0][2 * q]) + u2f(sv[1][2 * q]), u2f(sv[0][2 * q + 1]) + u2f(sv[1][2 * q + 1]));
          const u32 ca = cvtpk(u2f(sv[2][2 * q]) + u2f(sv[3][2 * q]), u2f(sv[2][2 * q + 1]) + u2f(sv[3][2 * q + 1]));
          const u32 c1_ = cvtpk(1.f + blo(ca), 1.f + bhi(ca)), tt = cvtpk(blo(A[q]) * blo(c1_), bhi(A[q]) * bhi(c1_));
          h[q] = cvtpk(blo(tt) + blo(sa), bhi(tt) + bhi(sa));
        }
        *(volatile LAS v2u*)(L + L_PC + a * 544 + lane * 8) = h;
        abar(1);
        const int ao = L_PC + (n >> 2) * 544 + (ak & 255) * 2;
        const v2u a0 = *(volatile LAS v2u*)(L + ao), a1 = *(volatile LAS v2u*)(L + ao + 64);
        A = v4u{a0.x, a0.y, a1.x, a1.y};
      }
      auto fold = [&](const v4f* d, int o) {
        v4f p;
#pragma unroll
        for (int cc = 0; cc < 4; ++cc) { const float own = n < 8 ? d[cc][0] : d[cc + 4][1], oth = n < 8 ? d[cc + 4][0] : d[cc][1]; p[cc] = own + dppf<ROR8>(oth); }
        *(volatile LAS v4f*)(L + o + a * 1024 + g * 256 + (n & 7) * 32 + (n >> 3) * 16) = p;
      };
      v4f d[8];
#pragma unroll
      for (int cc = 0; cc < 8; ++cc) d[cc] = mfma(A, B0[cc], v4f{0, 0, 0, 0});
      if (isL) {
        const v4u A2 = {dppu<XOR2>(A[0]), dppu<XOR2>(A[1]), dppu<XOR2>(A[2]), dppu<XOR2>(A[3])};
#pragma unroll
        for (int cc = 0; cc < 8; ++cc) d[cc] = mfma(A2, B1[cc], d[cc]);
      }
      if (isQ) {
        v4f e[8];
#pragma unroll
        for (int cc = 0; cc < 8; ++cc) e[cc] = mfma(A, B1[cc], v4f{0, 0, 0, 0});
        fold(e, L_PC + 4096);
      }
      fold(d, L_DP);
      abar(2);
      const int ro = ((isQ && (g & 1)) ? L_PC + 4096 : L_DP) + (isL ? g : a) * 256 + n * 16;
      const v4f q0 = *(volatile LAS v4f*)(L + ro), q1 = *(volatile LAS v4f*)(L + ro + 1024), q2 = *(volatile LAS v4f*)(L + ro + 2048), q3 = *(volatile LAS v4f*)(L + ro + 3072);
      const v4f w = (q0 + q1) + (q2 + q3);
      if (isL) {
        const u32 tn = tag >> (16 * (n & 1) + 4 * g);
        if (a == 0) st8(rs, WS_MODL + mt * 512 + n * 32 + g * 8, v2u{pkt(w[0], w[1], tb(tn)), pkt(w[2], w[3], tb(tn >> 2))});
      } else {
        const int o = isQ ? WS_Q + (x * 4 + a) * 4096 + jq * 512 + g * 256 : WS_SAT + (mt >> 4) * 4096 + (mt & 1) * 2048 + ((mt >> 1) & 7) * 256 + a * 8192;
        st16(rs, g < (isQ ? 2 : 1) ? o + n * 16 : OOB, tag4(w, t2));
      }
      lflag(F_PUB + 4 * a, 3);
      TS(3);

    } else {
      v2u r0[8], r1[8];
      ARGS();
      ep = ld4(rs, WS_EPOCH);
      ROLES
      pw = (isW ? PWW : PWF) / 10;
    if (isW) { wwp = (const unsigned char*)wo + (size_t)(xw * 512 + 128 * a + 8 * g) * 1024 + (64 * j + 4 * n) * 2; wws = 32 * 1024; wwi0 = 1024; wwi1 = 4 * 1024; }
    else { wwp = (const unsigned char*)(n < 8 ? wg : wu) + (size_t)(128 * a + 64 * (g >> 1) + 4 * (g & 1)) * 3584 + (32 * fs + 4 * (n & 7)) * 2; wws = 8 * 3584; wwi0 = 3584; wwi1 = 32 * 3584; }
      ldsinit();
      if (isF || isW) {
        const bool e0 = isW || FE >= 1, e1 = isW || FE >= 2;
        if (e0) load_w(r0, 0);
        if (e1) load_w(r1, 1);
        if (e0) { tr4(r0, Bw[0], isF); SCHED(); }
        const bool e2 = isW ? WE >= 3 : FE >= 3, e3 = isW ? WE == 4 : FE == 4;
        if (e2) load_w(r0, 2);
        if (QE && FE < 4 && isF) { tpw(TPF); qv = ld16(rs, QO); SCHED(); }
        if (e1) { tr4(r1, Bw[1], isF); SCHED(); }
        if (e3) load_w(r1, 3);
        if (e2) { tr4(r0, Bw[2], isF); SCHED(); }
        if (e3) { tr4(r1, Bw[3], isF); SCHED(); }
      }
      gettag();
      if (!isF) TS(1);
    }
    {
      const int qo = QO;
      const v4u t2 = tw2(tag, n & 3);
      lflag(F_TAG, tag);
      if (!(QE && isF)) {
        if (!(QE & 2) || !isQ) lwait4(F_KDONE, 2);
        tpw(TP);
        qv = ld16(rs, qo);
        TS(25);
      }
      qv ^= t2;
      if (UNLIKELY(any((or4(qv) & 3u) != 0))) {
        SPIN(GSPIN) {
          __builtin_amdgcn_s_sleep(D1);
          qv = ld16(rs, qo) ^ t2;
          if (!any((or4(qv) & 3u) != 0)) break;
        }
      }
      {
        float q0 = u2f(qv[0]), q1 = u2f(qv[1]), q2 = u2f(qv[2]), q3 = u2f(qv[3]);
        swap32(q0, q2); swap32(q1, q3);
        *(volatile LAS u32*)(L + L_Q + a * 272 + (lane & 31) * 8 + (lane >> 5) * 4) = cvtpk(q0 + q2, q1 + q3);
      }
      TS(4);
      abar(3);
      v4u qa[4];
#pragma unroll
      for (int ks = 0; ks < 4; ++ks) qa[ks] = *(volatile LAS v4u*)(L + L_Q + (n & 3) * 272 + ks * 64 + g * 16);
      lwait4(F_KDONE, 3);
      TS(5);
      float sc[8];
      v4u kf[2][4][4];
      auto ldk = [&](int u, int ks) {
#pragma unroll
        for (int t = 0; t < 4; ++t) kf[u][ks][t] = *(const LAS v4u*)(L + L_K + (128 * a + 64 * u + 16 * (n & 3) + 4 * t + (n >> 2)) * 256 + (((4 * ks + g) ^ n) * 16));
      };
#pragma unroll
      for (int c = 0; c < 8 / KB; ++c) ldk(c >> 2, c & 3);
      SCHED();
#pragma unroll
      for (int u = 0; u < 2; ++u) {
        v4f D[4];
#pragma unroll
        for (int t = 0; t < 4; ++t) D[t] = v4f{0, 0, 0, 0};
#pragma unroll
        for (int ks = 0; ks < 4; ++ks) {
#pragma unroll
          for (int t = 0; t < 4; ++t) D[t] = mfma(kf[u][ks][t], qa[ks], D[t]);
          const int c = 4 * u + ks + 8 / KB;
          if (c < 8) { ldk(c >> 2, c & 3); SCHED(); }
        }
#pragma unroll
        for (int r = 0; r < 4; r += 2) {
          const u32 w = cvtpk(sel4(n >> 2, D[0][r], D[1][r], D[2][r], D[3][r]), sel4(n >> 2, D[0][r + 1], D[1][r + 1], D[2][r + 1], D[3][r + 1]));
          const u32 w2 = cvtpk(blo(w) * 0.08838834764831845f, bhi(w) * 0.08838834764831845f);
          sc[4 * u + r] = blo(w2); sc[4 * u + r + 1] = bhi(w2);
        }
      }
      float m = fmaxf(fmaxf(fmaxf(sc[0], sc[1]), fmaxf(sc[2], sc[3])), fmaxf(fmaxf(sc[4], sc[5]), fmaxf(sc[6], sc[7])));
      m = fmaxf(m, dppf<ROR4>(m)); m = fmaxf(m, dppf<ROR8>(m)); m = max32(max16(m));
      *(volatile LAS float*)(L + L_MX + (n & 3) * 16 + a * 4) = m;
      abar(4);
      { const v4f t = *(volatile LAS v4f*)(L + L_MX + (n & 3) * 16); m = fmaxf(fmaxf(t[0], t[1]), fmaxf(t[2], t[3])); }
      float e[8];
#pragma unroll
      for (int q = 0; q < 8; ++q) e[q] = ex2((sc[q] - m) * LOG2E);
      *(volatile LAS v4u*)(L + L_P + ((((n >> 2) * 4 + a) * 4 + g) * 4 + (n & 3)) * 16) = v4u{cvtpk(e[0], e[1]), cvtpk(e[2], e[3]), cvtpk(e[4], e[5]), cvtpk(e[6], e[7])};
      lflag(F_PDONE + 4 * a, 1);
      float l = ((e[0] + e[1]) + (e[2] + e[3])) + ((e[4] + e[5]) + (e[6] + e[7]));
      l += dppf<ROR4>(l); l += dppf<ROR8>(l); l = sum32(sum16(l));
      *(volatile LAS float*)(L + L_MX + 64 + (n & 3) * 16 + a * 4) = l;
      lflag(F_ABAR + 4 * a, 5);
      if (a == 0) {
        lwait4(F_ABAR, 5);
        const v4f t = *(volatile LAS v4f*)(L + L_MX + 64 + (n & 3) * 16);
        st8(rs, lane < 4 ? WS_ML + x * 1024 + (b * 8 + j) * 32 + lane * 8 : OOB,
            v2u{__builtin_amdgcn_perm(f2u(m), tag, 0x07060100u), __builtin_amdgcn_perm(f2u(sum4(t)) + 128u, tag, 0x07060502u)});
      }
    }
    TS(6);
    ROLES
    if (WARM && isM) {
      const int t = a * 64 + lane;
      const int o = idx == 0 ? WS_OP + x * 32768 + t * 128 : idx == 1 ? WS_Y + ((t >> 3 & 15) * 8 + x) * 1024 + (t & 7) * 128 : WS_Z + (a * 56 + fs - j) * 1024 + lane * 128;
      while (now() - t0 < (idx == 0 ? TWA : idx == 1 ? TWB : TWC) / 10) __builtin_amdgcn_s_sleep(2);
      if ((WARM >> idx) & 1) { u32 u = ld4(rs, o); asm volatile("" :: "v"(u)); }
    }
    if (isW) {
      v4u tw = tw4(tag, g);
      u32 ty = tb(tag >> (2 * n));
      LAS unsigned char *pw1 = L + L_DP + a * 1024 + lane * 16, *pr0 = L + L_DP + (a & 1) * 512 + lane * 8;
      int yo = WS_Y + (((a & 2) * 4 + (a & 1) * 2 + (g >> 1)) * 8 + xw) * 1024 + j * 128 + (lane & 31) * 4;
      PIN(tw); PIN(ty); PIN(pw1); PIN(pr0); PIN(yo);
      const int mlo = WS_ML + xw * 1024 + (g * 8 + (n & 7)) * 32 + (a ^ (n >> 3)) * 8;
      v2u t;
      if (WE < 4) {
        v2u r0[8], r1[8];
        pw2 = 1;
        if (WE < 3) load_w(r0, 2);
        load_w(r1, 3);
        if (MLE) { t = ld8(rs, mlo); SCHED(); }
        if (WE < 3) { tr4(r0, Bw[2]); SCHED(); }
        tr4(r1, Bw[3]); SCHED();
        TS(7);
      }
      v32u B01, B23;
#pragma unroll
      for (int i = 0; i < 32; ++i) { B01[i] = Bw[i >> 4][(i >> 2) & 3][i & 3]; B23[i] = Bw[2 + (i >> 4)][(i >> 2) & 3][i & 3]; }
      asm volatile("" : "+{v[64:95]}"(B01), "+{v[96:127]}"(B23), "+{v[128:131]}"(tw));
      v8f fr;
      SPIN(GSPIN) {
        if (!MLE || WE == 4 || sp) t = ld8(rs, mlo);
        const float m = bhi(t.x);
        float M = fmaxf(m, dppf<XOR1>(m)); M = fmaxf(M, dppf<XOR2>(M)); M = fmaxf(M, dppf<0x141>(M));
        const float w = ex2((m - M) * LOG2E);
        float l = untag(t.y) * w;
        l += dppf<XOR1>(l); l += dppf<XOR2>(l); l += dppf<0x141>(l);
        const float f = w / l;
        fr[0] = dppf<NBC + 0>(f); fr[1] = dppf<NBC + 1>(f); fr[2] = dppf<NBC + 2>(f); fr[3] = dppf<NBC + 3>(f);
        fr[4] = dppf<NBC + 4>(f); fr[5] = dppf<NBC + 5>(f); fr[6] = dppf<NBC + 6>(f); fr[7] = dppf<NBC + 7>(f);
        if (!any(__builtin_amdgcn_perm(t.y, t.x, 0x0c040100u) != (tag & 0xFFFFFFu))) break;
        __builtin_amdgcn_s_sleep(DM);
      }
      asm volatile("" : "+{v[132:139]}"(fr));
      TS(12);
      if (!EL) lwait4(F_KDONE, 5);
      if (TEL) tpw(TEL);
      if (TWG) { while (now() - t0 < TWG / 10) ; }
      TS(9);
      CT(0);
      v4f tt;
      v32u P[1];
      u32 lcnt;
      {
        const int ooff = WS_OP + (xw * 4 + a) * 8192 + n * 64 + g * 16;
        u32 d0, d1; u64 sv;
        asm volatile(
        "s_mov_b32 %1, 0\n\ts_mov_b32 %2, 0\n\ts_mov_b32 %3, %10\n\ts_mov_b64 %4, exec\n\tbuffer_load_dwordx4 v[140:143], %6, %7, 0 offen sc1\n\t"
        "buffer_load_dwordx4 v[144:147], %6, %7, 0 offen offset:2048 sc1\n\tbuffer_load_dwordx4 v[148:151], %6, %7, %8 offen sc1\n\t"
        "buffer_load_dwordx4 v[152:155], %6, %7, %8 offen offset:2048 sc1\n\t.if %12\n\tv_not_b32 v156, v128\n\ts_mov_b64 exec, 1\n\t.endif\n\tbuffer_load_dwordx4 v[156:159], %6, %7, 0 offen offset:1024 sc1\n\t"
        "buffer_load_dwordx4 v[160:163], %6, %7, 0 offen offset:3072 sc1\n\tbuffer_load_dwordx4 v[164:167], %6, %7, %8 offen offset:1024 sc1\n\t"
        "buffer_load_dwordx4 v[168:171], %6, %7, %8 offen offset:3072 sc1\n\ts_mov_b64 exec, %4\n\tv_mov_b32 v246, v128\n\tv_mov_b32 v247, v129\n\tv_mov_b32 v248, v130\n\tv_mov_b32 v249, v131\n\t"
        "v_mov_b32 v250, v128\n\tv_mov_b32 v251, v129\n\tv_mov_b32 v252, v130\n\tv_mov_b32 v253, v131\n\ts_branch 10f\n\t11:\n\ts_mov_b64 s[92:93], 0\n\ts_waitcnt vmcnt(3)\n\t"
        "v_xor_b32 v156, v156, v250\n\tv_xor_b32 v157, v157, v251\n\tv_xor_b32 v158, v158, v252\n\tv_xor_b32 v159, v159, v253\n\ts_waitcnt vmcnt(1)\n\t"
        "v_xor_b32 v160, v160, v250\n\tv_xor_b32 v161, v161, v251\n\tv_xor_b32 v162, v162, v252\n\tv_xor_b32 v163, v163, v253\n\tv_xor_b32 v164, v164, v250\n\t"
        "v_xor_b32 v165, v165, v251\n\tv_xor_b32 v166, v166, v252\n\tv_xor_b32 v167, v167, v253\n\ts_waitcnt vmcnt(0)\n\tv_xor_b32 v168, v168, v250\n\t"
        "v_xor_b32 v169, v169, v251\n\tv_xor_b32 v170, v170, v252\n\tv_xor_b32 v171, v171, v253\n\ts_branch 16f\n\t12:\n\ts_nop 2\n\tv_mul_f32 v213, v200, v136\n\t"
        "v_fmac_f32 v213, v201, v137\n\tv_fmac_f32 v213, v202, v138\n\tv_fmac_f32 v213, v203, v139\n\ts_mov_b32 %2, 1\n\ts_branch 14f\n\t13:\n\ts_mov_b64 vcc, 0\n\t"
        "s_cmp_lg_u64 s[92:93], 0\n\ts_cbranch_scc1 14f\n\tv_mul_f32 v206, v172, v132\n\tv_fmac_f32 v206, v173, v133\n\tv_fmac_f32 v206, v174, v134\n\t"
        "v_fmac_f32 v206, v175, v135\n\tv_mul_f32 v207, v176, v132\n\tv_fmac_f32 v207, v177, v133\n\tv_fmac_f32 v207, v178, v134\n\tv_fmac_f32 v207, v179, v135\n\t"
        "v_mul_f32 v208, v180, v132\n\tv_fmac_f32 v208, v181, v133\n\tv_fmac_f32 v208, v182, v134\n\tv_fmac_f32 v208, v183, v135\n\tv_mul_f32 v209, v184, v132\n\t"
        "v_fmac_f32 v209, v185, v133\n\tv_fmac_f32 v209, v186, v134\n\tv_fmac_f32 v209, v187, v135\n\tv_add_f32 v206, v206, v210\n\tv_add_f32 v207, v207, v211\n\t"
        "v_add_f32 v208, v208, v212\n\tv_add_f32 v209, v209, v213\n\ts_branch 99f\n\t14:\n\ts_or_b32 s92, s92, s93\n\ts_or_b32 s94, vcc_lo, vcc_hi\n\ts_lshr_b32 s93, s92, 16\n\t"
        "s_lshr_b32 s95, s94, 16\n\ts_or_b32 s92, s92, s93\n\ts_cselect_b32 %1, %1, 1\n\ts_or_b32 s94, s94, s95\n\ts_pack_ll_b32_b16 s93, s92, s92\n\t"
        "s_pack_ll_b32_b16 s95, s94, s94\n\ts_or_b32 s92, s93, 1\n\ts_or_b32 s94, s95, 1\n\ts_sub_u32 %3, %3, 1\n\ts_cbranch_scc1 99f\n\t.if %11\n\ts_sleep %11\n\t.endif\n\t"
        ".if %12\n\t17:\n\tds_read_b128 v[246:249], %17\n\ts_sub_u32 %3, %3, 1\n\ts_cbranch_scc1 99f\n\ts_waitcnt lgkmcnt(0)\n\tv_min_u32 v246, v246, v247\n\tv_min3_u32 v246, v246, v248, v249\n\t"
        "v_cmp_gt_u32_e32 vcc, %12, v246\n\ts_cbranch_vccnz 17b\n\t.endif\n\t"
        "s_mov_b64 exec, s[92:93]\n\tbuffer_load_dwordx4 v[140:143], %6, %7, 0 offen sc1\n\tbuffer_load_dwordx4 v[144:147], %6, %7, 0 offen offset:2048 sc1\n\t"
        "buffer_load_dwordx4 v[148:151], %6, %7, %8 offen sc1\n\tbuffer_load_dwordx4 v[152:155], %6, %7, %8 offen offset:2048 sc1\n\ts_mov_b64 exec, s[94:95]\n\t"
        "buffer_load_dwordx4 v[156:159], %6, %7, 0 offen offset:1024 sc1\n\tbuffer_load_dwordx4 v[160:163], %6, %7, 0 offen offset:3072 sc1\n\t"
        "buffer_load_dwordx4 v[164:167], %6, %7, %8 offen offset:1024 sc1\n\tbuffer_load_dwordx4 v[168:171], %6, %7, %8 offen offset:3072 sc1\n\ts_mov_b64 exec, %4\n\t"
        "v_cndmask_b32_e64 v246, 0, v128, s[92:93]\n\tv_cndmask_b32_e64 v247, 0, v129, s[92:93]\n\tv_cndmask_b32_e64 v248, 0, v130, s[92:93]\n\t"
        "v_cndmask_b32_e64 v249, 0, v131, s[92:93]\n\tv_cndmask_b32_e64 v250, 0, v128, s[94:95]\n\tv_cndmask_b32_e64 v251, 0, v129, s[94:95]\n\t"
        "v_cndmask_b32_e64 v252, 0, v130, s[94:95]\n\tv_cndmask_b32_e64 v253, 0, v131, s[94:95]\n\t10:\n\ts_cmp_lg_u32 %1, 0\n\ts_cbranch_scc1 11b\n\ts_waitcnt vmcnt(7)\n\t"
        "v_xor_b32 v140, v140, v246\n\tv_xor_b32 v141, v141, v247\n\tv_xor_b32 v142, v142, v248\n\tv_xor_b32 v143, v143, v249\n\ts_waitcnt vmcnt(6)\n\t"
        "v_xor_b32 v144, v144, v246\n\tv_mfma_f32_16x16x32_bf16 v[172:175], v[140:143], v[64:67], 0\n\tv_xor_b32 v145, v145, v247\n\tv_xor_b32 v146, v146, v248\n\t"
        "v_xor_b32 v147, v147, v249\n\tv_mfma_f32_16x16x32_bf16 v[176:179], v[140:143], v[68:71], 0\n\tv_or3_b32 v204, v140, v141, v142\n\ts_waitcnt vmcnt(5)\n\t"
        "v_xor_b32 v148, v148, v246\n\tv_mfma_f32_16x16x32_bf16 v[180:183], v[140:143], v[72:75], 0\n\tv_xor_b32 v149, v149, v247\n\tv_xor_b32 v150, v150, v248\n\t"
        "v_mfma_f32_16x16x32_bf16 v[184:187], v[140:143], v[76:79], 0\n\tv_xor_b32 v151, v151, v249\n\tv_or3_b32 v204, v204, v143, v144\n\t"
        "v_mfma_f32_16x16x32_bf16 v[172:175], v[144:147], v[80:83], v[172:175]\n\ts_waitcnt vmcnt(4)\n\tv_xor_b32 v152, v152, v246\n\tv_xor_b32 v153, v153, v247\n\t"
        "v_mfma_f32_16x16x32_bf16 v[176:179], v[144:147], v[84:87], v[176:179]\n\tv_xor_b32 v154, v154, v248\n\tv_xor_b32 v155, v155, v249\n\t"
        "v_mfma_f32_16x16x32_bf16 v[180:183], v[144:147], v[88:91], v[180:183]\n\tv_or3_b32 v204, v204, v145, v146\n\tv_or3_b32 v204, v204, v147, v148\n\t"
        "v_mfma_f32_16x16x32_bf16 v[184:187], v[144:147], v[92:95], v[184:187]\n\ts_waitcnt vmcnt(2)\n\tv_xor_b32 v156, v156, v250\n\tv_xor_b32 v157, v157, v251\n\t"
        "v_mfma_f32_16x16x32_bf16 v[172:175], v[148:151], v[96:99], v[172:175]\n\tv_xor_b32 v158, v158, v252\n\tv_xor_b32 v159, v159, v253\n\tv_or3_b32 v204, v204, v149, v150\n\t"
        "v_mfma_f32_16x16x32_bf16 v[176:179], v[148:151], v[100:103], v[176:179]\n\tv_xor_b32 v160, v160, v250\n\tv_xor_b32 v161, v161, v251\n\t"
        "v_mfma_f32_16x16x32_bf16 v[180:183], v[148:151], v[104:107], v[180:183]\n\tv_xor_b32 v162, v162, v252\n\tv_xor_b32 v163, v163, v253\n\t"
        "v_or3_b32 v204, v204, v151, v152\n\tv_mfma_f32_16x16x32_bf16 v[184:187], v[148:151], v[108:111], v[184:187]\n\ts_waitcnt vmcnt(0)\n\tv_xor_b32 v164, v164, v250\n\t"
        "v_xor_b32 v165, v165, v251\n\tv_mfma_f32_16x16x32_bf16 v[172:175], v[152:155], v[112:115], v[172:175]\n\tv_xor_b32 v166, v166, v252\n\tv_xor_b32 v167, v167, v253\n\t"
        "v_or3_b32 v204, v204, v153, v154\n\tv_mfma_f32_16x16x32_bf16 v[176:179], v[152:155], v[116:119], v[176:179]\n\tv_xor_b32 v168, v168, v250\n\t"
        "v_xor_b32 v169, v169, v251\n\tv_bitop3_b32 v204, v204, %9, v155 bitop3:0xc8\n\tv_mfma_f32_16x16x32_bf16 v[180:183], v[152:155], v[120:123], v[180:183]\n\t"
        "v_xor_b32 v170, v170, v252\n\tv_xor_b32 v171, v171, v253\n\tv_cmp_ne_u32_e64 s[92:93], 0, v204\n\t"
        "v_mfma_f32_16x16x32_bf16 v[184:187], v[152:155], v[124:127], v[184:187]\n\ts_cmp_lg_u32 %2, 0\n\ts_cbranch_scc1 13b\n\t16:\n\t"
        "v_mfma_f32_16x16x32_bf16 v[188:191], v[156:159], v[64:67], 0\n\tv_or3_b32 v205, v156, v157, v158\n\tv_or3_b32 v205, v205, v159, v160\n\t"
        "v_or3_b32 v205, v205, v161, v162\n\tv_mfma_f32_16x16x32_bf16 v[188:191], v[160:163], v[80:83], v[188:191]\n\tv_or3_b32 v205, v205, v163, v164\n\t"
        "v_or3_b32 v205, v205, v165, v166\n\tv_or3_b32 v205, v205, v167, v168\n\tv_mfma_f32_16x16x32_bf16 v[188:191], v[164:167], v[96:99], v[188:191]\n\t"
        "v_or3_b32 v205, v205, v169, v170\n\tv_bitop3_b32 v205, v205, %9, v171 bitop3:0xc8\n\tv_cmp_ne_u32_e32 vcc, 0, v205\n\t"
        "v_mfma_f32_16x16x32_bf16 v[188:191], v[168:171], v[112:115], v[188:191]\n\ts_cbranch_vccnz 14b\n\tv_mul_f32 v206, v172, v132\n\tv_fmac_f32 v206, v173, v133\n\t"
        "v_mfma_f32_16x16x32_bf16 v[192:195], v[156:159], v[68:71], 0\n\tv_fmac_f32 v206, v174, v134\n\tv_fmac_f32 v206, v175, v135\n\tv_mul_f32 v207, v176, v132\n\t"
        "v_mfma_f32_16x16x32_bf16 v[192:195], v[160:163], v[84:87], v[192:195]\n\tv_fmac_f32 v207, v177, v133\n\tv_fmac_f32 v207, v178, v134\n\t"
        "v_mfma_f32_16x16x32_bf16 v[192:195], v[164:167], v[100:103], v[192:195]\n\tv_fmac_f32 v207, v179, v135\n\tv_mul_f32 v208, v180, v132\n\tv_fmac_f32 v208, v181, v133\n\t"
        "v_mfma_f32_16x16x32_bf16 v[192:195], v[168:171], v[116:119], v[192:195]\n\tv_mul_f32 v210, v188, v136\n\tv_fmac_f32 v210, v189, v137\n\t"
        "v_mfma_f32_16x16x32_bf16 v[196:199], v[156:159], v[72:75], 0\n\tv_fmac_f32 v210, v190, v138\n\tv_fmac_f32 v210, v191, v139\n\tv_fmac_f32 v208, v182, v134\n\t"
        "v_mfma_f32_16x16x32_bf16 v[196:199], v[160:163], v[88:91], v[196:199]\n\tv_fmac_f32 v208, v183, v135\n\tv_add_f32 v206, v206, v210\n\t"
        "v_mfma_f32_16x16x32_bf16 v[196:199], v[164:167], v[104:107], v[196:199]\n\tv_mul_f32 v209, v184, v132\n\tv_fmac_f32 v209, v185, v133\n\tv_fmac_f32 v209, v186, v134\n\t"
        "v_mfma_f32_16x16x32_bf16 v[196:199], v[168:171], v[120:123], v[196:199]\n\tv_mul_f32 v211, v192, v136\n\tv_fmac_f32 v211, v193, v137\n\t"
        "v_mfma_f32_16x16x32_bf16 v[200:203], v[156:159], v[76:79], 0\n\tv_fmac_f32 v211, v194, v138\n\tv_fmac_f32 v211, v195, v139\n\tv_fmac_f32 v209, v187, v135\n\t"
        "v_mfma_f32_16x16x32_bf16 v[200:203], v[160:163], v[92:95], v[200:203]\n\tv_add_f32 v207, v207, v211\n\t"
        "v_mfma_f32_16x16x32_bf16 v[200:203], v[164:167], v[108:111], v[200:203]\n\tv_mul_f32 v212, v196, v136\n\tv_fmac_f32 v212, v197, v137\n\t"
        "v_mfma_f32_16x16x32_bf16 v[200:203], v[168:171], v[124:127], v[200:203]\n\tv_fmac_f32 v212, v198, v138\n\tv_fmac_f32 v212, v199, v139\n\tv_add_f32 v208, v208, v212\n\t"
        "s_cmp_lg_u64 s[92:93], 0\n\ts_cbranch_scc1 12b\n\ts_nop 2\n\tv_mul_f32 v213, v200, v136\n\tv_fmac_f32 v213, v201, v137\n\tv_fmac_f32 v213, v202, v138\n\t"
        "v_fmac_f32 v213, v203, v139\n\tv_add_f32 v209, v209, v213\n\t99:\n\t"
        : "=&{v[206:209]}"(tt), "=&s"(d0), "=&s"(d1), "=&s"(lcnt), "=&s"(sv), "=&{v[140:171]}"(P[0])
        : "v"(ooff), "s"(rd4), "s"(4096), "s"(LSB), "i"(GSPIN), "i"(D3), "i"(EL), "{v[64:95]}"(B01), "{v[96:127]}"(B23), "{v[128:131]}"(tw), "{v[132:139]}"(fr), "v"(L + F_KDONE)
        : "memory", "vcc", "scc", "s92", "s93", "s94", "s95", "v172", "v173", "v174", "v175", "v176", "v177", "v178", "v179", "v180", "v181", "v182", "v183", "v184", "v185", "v186", "v187", "v188", "v189", "v190", "v191", "v192", "v193", "v194", "v195", "v196", "v197", "v198", "v199", "v200", "v201", "v202", "v203", "v204", "v205", "v210", "v211", "v212", "v213", "v246", "v247", "v248", "v249", "v250", "v251", "v252", "v253");
      }
      TS(10);
      CT(1);
      *(LAS v4f*)pw1 = tt;
      wgbar();
      CT(2);
      const v2f y0 = *(volatile LAS v2f*)pr0, y1 = *(volatile LAS v2f*)(pr0 + 1024), y2 = *(volatile LAS v2f*)(pr0 + 2048), y3 = *(volatile LAS v2f*)(pr0 + 3072);
      SCHED();
      const v2f y = (y0 + y1) + (y2 + y3);
      const u32 yw = pkt(y[0], y[1], ty);
      st4(rs, yo, yw);
      st4(rs, yo, yw, 32768);
      CT(3);
      TS(11);
      CTOUT(3);
      asm volatile("s_waitcnt vmcnt(0)" :: "{v[140:171]}"(P[0]));
    } else if (isF) {
      auto packA = [&](u32 a01, u32 a23, u32 b01, u32 b23) {
        return v4u{upd<0x110 + 4, 0xa>(a01, b01), upd<0x110 + 4, 0xa>(a23, b23), upd<SHL + 4, 0x5>(b01, a01), upd<SHL + 4, 0x5>(b23, a23)};
      };
      auto shl = [&](v4u A, auto c) { return v4u{dppu<SHL + c.value>(A[0]), dppu<SHL + c.value>(A[1]), dppu<SHL + c.value>(A[2]), dppu<SHL + c.value>(A[3])}; };
      const int fcol = 128 * a + 64 * (g >> 1) + 8 * (nn & 7) + 4 * (g & 1);
      const int id = SEP ? 8 * xh + j : 4 * x + j, bt = id >> 3;
      float xr[2][4], ga[2][4], xc[2][4], gc[2][4];
      v2u gbp;
      const int wo_ = a * 1024 + g * 256 + (n ^ ((g & 1) << 2)) * 16;
      const bool red = (SEP ? b == RB : j < 4) && a == ((id & 7) >> 1);
      const int ho = (n >> 2) * 256 + ((8 * (n & 1) + 2 * g + ((n >> 1) & 1)) ^ (n & 4)) * 16;
      v4u S0, S1, tw = tw4(tag, nn & 3);
      LAS unsigned char *pw0 = L + L_ST + (2 * (n >> 3) + (g & 1)) * 32 + a * 8 + (g >> 1) * 4, *pw1 = L + L_DP + wo_, *pr0 = L + L_DP + ho, *pr1 = L + L_ST + (n >> 2) * 32;
      int zo = WS_Z + (g * 56 + fs) * 1024 + a * 256 + nn * 16;
      int yoff = WS_Y + (((SEP ? xh : x & (RY - 1)) * 4 + 2 * (n >> 3)) * 8 + g) * 1024 + a * 256 + (nn & 7) * 16;
      v4f Cw, Bq;
      v2f Be;
      {
        v2u rr[4][8]; v4u rd[8];
        v4u mm[4];
        const v4u tm = tw4(tag, 2 * (g & 1) + (n >> 3));
        const int mo = WS_MODL + fcol * 8 + (n >> 3) * 16;
        auto ldm = [&]() {
#pragma unroll
          for (int i = 0; i < 3; ++i) mm[i] = ld16(rs, mo + i * 4096);
          mm[3] = ld16(rs, red ? mo + 3 * 4096 : mo);
        };
        const v2u xf[2] = {*(const v2u*)(xin + (2 * (n >> 3)) * 512 + fcol), *(const v2u*)(xin + (2 * (n >> 3) + 1) * 512 + fcol)};
        if (TML) tpw(TML);
        ldm();
        {
          const u32 one = (n & 1) ? 0x3F800000u : 0x3F80u;
#pragma unroll
          for (int m = 0; m < 4; ++m) { S0[m] = (n >> 1) == m ? one : 0u; S1[m] = (n >> 1) == m + 4 ? one : 0u; }
        }
        const unsigned char* dp = (const unsigned char*)wd + (size_t)(32 * fs + 8 * g) * 1024 + (128 * a + 8 * nn) * 2;
        LAS unsigned char *pc0 = L + L_PC + wo_, *pc1 = L + L_PC + ho;
        PIN(S0); PIN(S1); PIN(tw); PIN(pw0); PIN(pw1); PIN(pr0); PIN(pr1); PIN(zo); PIN(yoff); PIN(dp); PIN(pc0); PIN(pc1);
        SCHED();
        SPIN(GSPIN) {
#pragma unroll
          for (int i = 0; i < 4; ++i) mm[i] ^= tm;
          if (LIKELY(!any((((or4(mm[0]) | or4(mm[1])) | (or4(mm[2]) | or4(mm[3]))) & LSB) != 0))) break;
          __builtin_amdgcn_s_sleep(4);
          ldm();
        }
        u32 c01[2], c23[2];
#pragma unroll
        for (int t = 0; t < 2; ++t) {
          c01[t] = cvtpk(1.f + blo(mm[2][2 * t]), 1.f + bhi(mm[2][2 * t])); c23[t] = cvtpk(1.f + blo(mm[2][2 * t + 1]), 1.f + bhi(mm[2][2 * t + 1]));
          const float c1[4] = {blo(c01[t]), bhi(c01[t]), blo(c23[t]), bhi(c23[t])};
          xr[t][0] = blo(xf[t].x); xr[t][1] = bhi(xf[t].x); xr[t][2] = blo(xf[t].y); xr[t][3] = bhi(xf[t].y);
          ga[t][0] = blo(mm[0][2 * t]); ga[t][1] = bhi(mm[0][2 * t]); ga[t][2] = blo(mm[0][2 * t + 1]); ga[t][3] = bhi(mm[0][2 * t + 1]);
#pragma unroll
          for (int r = 0; r < 4; ++r) { xc[t][r] = xr[t][r] * c1[r]; gc[t][r] = ga[t][r] * c1[r]; PIN(xc[t][r]); PIN(gc[t][r]); PIN(xr[t][r]); PIN(ga[t][r]); }
        }
        gbp = (bt & 1) ? v2u{mm[3].z, mm[3].w} : v2u{mm[3].x, mm[3].y};
        PIN(gbp);
        v4u Am[2][4];
        Am[0][0] = packA(c01[0], c23[0], c01[1], c23[1]); Am[1][0] = packA(mm[1].x, mm[1].y, mm[1].z, mm[1].w);
        v4f dm[2][4];
#pragma unroll
        for (int t = 0; t < 2; ++t) {
          Am[t][1] = shl(Am[t][0], IC<1>{}); Am[t][2] = shl(Am[t][0], IC<2>{}); Am[t][3] = shl(Am[t][0], IC<3>{});
#pragma unroll
          for (int sl = 0; sl < FE; ++sl)
#pragma unroll
            for (int cc = 0; cc < 4; ++cc) dm[t][cc] = mfma(Am[t][sl], Bw[sl][cc], sl ? dm[t][cc] : v4f{0, 0, 0, 0});
#pragma unroll
          for (int cc = 0; cc < 4; ++cc) PIN(dm[t][cc]);
#pragma unroll
          for (int sl = FE; sl < 4; ++sl) PIN(Am[t][sl]);
        }
        SCHED();
        TS(7);
        pw2 = 2;
        if (F3K) lwait4(F_KDONE, F3K);
#pragma unroll
        for (int sl = FE; sl < 4; ++sl) load_w(rr[sl], sl, PF2 != 0);
        lwait4(F_KDONE, DK);
        TS(9);
#pragma unroll
        for (int i = 0; i < 8; ++i) rd[i] = __builtin_nontemporal_load((const v4u*)(dp + i * 1024));
        SCHED();
#pragma unroll
        for (int sl = FE; sl < 4; ++sl) {
          tr4(rr[sl], Bw[sl], true); SCHED();
#pragma unroll
          for (int t = 0; t < 2; ++t)
#pragma unroll
            for (int cc = 0; cc < 4; ++cc) dm[t][cc] = mfma(Am[t][sl], Bw[sl][cc], dm[t][cc]);
        }
#pragma unroll
        for (int t = 0; t < 2; ++t) *(volatile LAS v4f*)(pc0 + t * 4096) = v4f{dm[t][0][0], dm[t][1][0], dm[t][2][0], dm[t][3][0]};
        wgbar();
        auto sum4w = [&](LAS unsigned char* o) { return (*(LAS v4f*)o + *(LAS v4f*)(o + 1024)) + (*(LAS v4f*)(o + 2048) + *(LAS v4f*)(o + 3072)); };
        Cw = sum4w(pc1) * (-1.f / 512.f); Bq = sum4w(pc1 + 4096);
        Be = v2f{Bq[0] * -LOG2E, Bq[1] * -LOG2E};
        PIN(Cw); PIN(Bq); PIN(Be);
        SCHED();
        tr8(rd, Bd);
      }
      TS(8);
      v32u P[1];
      if (TY) { while (now() - t0 < TY / 10) __builtin_amdgcn_s_sleep(1); }
      v4f yt[2];
      u32 cnt;
      {
        u32 c0, c1, c2, c3, c4; u64 sv;
        CT(0);
        asm volatile(
        "s_mov_b32 %7, %22\n\ts_mov_b64 %8, exec\n\tbuffer_load_dwordx4 v[160:163], %16, %17, 0 offen sc1\n\tbuffer_load_dwordx4 v[164:167], %16, %17, %19 offen sc1\n\t"
        "buffer_load_dwordx4 v[168:171], %16, %17, 0 offen offset:128 sc1\n\tbuffer_load_dwordx4 v[172:175], %16, %17, %19 offen offset:128 sc1\n\t"
        "buffer_load_dwordx4 v[176:179], %16, %17, %18 offen sc1\n\tbuffer_load_dwordx4 v[180:183], %16, %17, %20 offen sc1\n\t"
        "buffer_load_dwordx4 v[184:187], %16, %17, %18 offen offset:128 sc1\n\tbuffer_load_dwordx4 v[188:191], %16, %17, %20 offen offset:128 sc1\n\ts_mov_b64 s[76:77], exec\n\t"
        "s_mov_b64 s[78:79], exec\n\ts_mov_b64 s[80:81], exec\n\ts_mov_b64 s[82:83], exec\n\ts_branch 10f\n\t14:\n\t"
        "v_or_b32_dpp %2, %2, %2 quad_perm:[1,0,3,2] row_mask:0xf bank_mask:0xf\n\tv_or_b32_dpp %3, %3, %3 quad_perm:[1,0,3,2] row_mask:0xf bank_mask:0xf\n\t"
        "v_or_b32_dpp %4, %4, %4 quad_perm:[1,0,3,2] row_mask:0xf bank_mask:0xf\n\tv_or_b32_dpp %5, %5, %5 quad_perm:[1,0,3,2] row_mask:0xf bank_mask:0xf\n\t"
        "v_or_b32_dpp %2, %2, %2 quad_perm:[2,3,0,1] row_mask:0xf bank_mask:0xf\n\tv_or_b32_dpp %3, %3, %3 quad_perm:[2,3,0,1] row_mask:0xf bank_mask:0xf\n\t"
        "v_or_b32_dpp %4, %4, %4 quad_perm:[2,3,0,1] row_mask:0xf bank_mask:0xf\n\tv_or_b32_dpp %5, %5, %5 quad_perm:[2,3,0,1] row_mask:0xf bank_mask:0xf\n\t"
        "v_and_b32 %2, %21, %2\n\tv_and_b32 %3, %21, %3\n\tv_and_b32 %4, %21, %4\n\tv_and_b32 %5, %21, %5\n\tv_cmp_ne_u32_e64 s[76:77], 0, %2\n\t"
        "v_cmp_ne_u32_e64 s[78:79], 0, %3\n\tv_cmp_ne_u32_e64 s[80:81], 0, %4\n\tv_cmp_ne_u32_e64 s[82:83], 0, %5\n\ts_sub_u32 %7, %7, 1\n\ts_cbranch_scc1 99f\n\t.if %23\n\t"
        "s_sleep %23\n\t.endif\n\ts_or_b32 s76, s76, 1\n\ts_mov_b64 exec, s[76:77]\n\tbuffer_load_dwordx4 v[160:163], %16, %17, 0 offen sc1\n\t"
        "buffer_load_dwordx4 v[164:167], %16, %17, %19 offen sc1\n\ts_or_b32 s78, s78, 1\n\ts_mov_b64 exec, s[78:79]\n\t"
        "buffer_load_dwordx4 v[168:171], %16, %17, 0 offen offset:128 sc1\n\tbuffer_load_dwordx4 v[172:175], %16, %17, %19 offen offset:128 sc1\n\ts_or_b32 s80, s80, 1\n\t"
        "s_mov_b64 exec, s[80:81]\n\tbuffer_load_dwordx4 v[176:179], %16, %17, %18 offen sc1\n\tbuffer_load_dwordx4 v[180:183], %16, %17, %20 offen sc1\n\ts_or_b32 s82, s82, 1\n\t"
        "s_mov_b64 exec, s[82:83]\n\tbuffer_load_dwordx4 v[184:187], %16, %17, %18 offen offset:128 sc1\n\tbuffer_load_dwordx4 v[188:191], %16, %17, %20 offen offset:128 sc1\n\t"
        "10:\n\ts_mov_b64 exec, s[76:77]\n\ts_waitcnt vmcnt(6)\n\tv_xor_b32 v160, v160, %12\n\tv_xor_b32 v161, v161, %13\n\tv_xor_b32 v162, v162, %14\n\t"
        "v_xor_b32 v163, v163, %15\n\tv_xor_b32 v164, v164, %12\n\tv_xor_b32 v165, v165, %13\n\tv_xor_b32 v166, v166, %14\n\tv_xor_b32 v167, v167, %15\n\ts_mov_b64 exec, %8\n\t"
        "v_mfma_f32_16x16x32_bf16 %0, %10, v[160:163], 0\n\tv_or3_b32 %2, v160, v161, v162\n\tv_mfma_f32_16x16x32_bf16 %1, %10, v[164:167], 0\n\tv_or3_b32 %2, %2, v163, v164\n\t"
        "v_or3_b32 %2, %2, v165, v166\n\tv_or_b32 %2, %2, v167\n\ts_mov_b64 exec, s[78:79]\n\ts_waitcnt vmcnt(4)\n\tv_xor_b32 v168, v168, %12\n\tv_xor_b32 v169, v169, %13\n\t"
        "v_xor_b32 v170, v170, %14\n\tv_xor_b32 v171, v171, %15\n\tv_xor_b32 v172, v172, %12\n\tv_xor_b32 v173, v173, %13\n\tv_xor_b32 v174, v174, %14\n\t"
        "v_xor_b32 v175, v175, %15\n\ts_mov_b64 exec, %8\n\tv_mfma_f32_16x16x32_bf16 %0, %11, v[168:171], %0\n\tv_or3_b32 %3, v168, v169, v170\n\t"
        "v_mfma_f32_16x16x32_bf16 %1, %11, v[172:175], %1\n\tv_or3_b32 %3, %3, v171, v172\n\tv_or3_b32 %3, %3, v173, v174\n\tv_or_b32 %3, %3, v175\n\ts_mov_b64 exec, s[80:81]\n\t"
        "s_waitcnt vmcnt(2)\n\tv_xor_b32 v176, v176, %12\n\tv_xor_b32 v177, v177, %13\n\tv_xor_b32 v178, v178, %14\n\tv_xor_b32 v179, v179, %15\n\tv_xor_b32 v180, v180, %12\n\t"
        "v_xor_b32 v181, v181, %13\n\tv_xor_b32 v182, v182, %14\n\tv_xor_b32 v183, v183, %15\n\ts_mov_b64 exec, %8\n\tv_mfma_f32_16x16x32_bf16 %0, %10, v[176:179], %0\n\t"
        "v_or3_b32 %4, v176, v177, v178\n\tv_mfma_f32_16x16x32_bf16 %1, %10, v[180:183], %1\n\tv_or3_b32 %4, %4, v179, v180\n\tv_or3_b32 %4, %4, v181, v182\n\t"
        "v_or_b32 %4, %4, v183\n\tv_or3_b32 %6, %2, %3, %4\n\ts_mov_b64 exec, s[82:83]\n\ts_waitcnt vmcnt(0)\n\tv_xor_b32 v184, v184, %12\n\tv_xor_b32 v185, v185, %13\n\t"
        "v_xor_b32 v186, v186, %14\n\tv_xor_b32 v187, v187, %15\n\tv_xor_b32 v188, v188, %12\n\tv_xor_b32 v189, v189, %13\n\tv_xor_b32 v190, v190, %14\n\t"
        "v_xor_b32 v191, v191, %15\n\ts_mov_b64 exec, %8\n\tv_mfma_f32_16x16x32_bf16 %0, %11, v[184:187], %0\n\tv_or3_b32 %5, v184, v185, v186\n\t"
        "v_mfma_f32_16x16x32_bf16 %1, %11, v[188:191], %1\n\tv_or3_b32 %5, %5, v187, v188\n\tv_or3_b32 %5, %5, v189, v190\n\tv_or_b32 %5, %5, v191\n\t"
        "v_bitop3_b32 %6, %6, %21, %5 bitop3:0xc8\n\tv_cmp_ne_u32_e32 vcc, 0, %6\n\ts_cbranch_vccnz 14b\n\ts_nop 0\n\t99:\n\ts_nop 0\n\t"
        : "=&v"(yt[0]), "=&v"(yt[1]), "=&v"(c0), "=&v"(c1), "=&v"(c2), "=&v"(c3), "=&v"(c4), "=&s"(cnt), "=&s"(sv), "=&{v[160:191]}"(P[0])
        : "v"(S0), "v"(S1), "v"(tw.x), "v"(tw.y), "v"(tw.z), "v"(tw.w), "v"(yoff), "s"(rd4), "s"(4096), "s"(8192), "s"(12288), "s"(LSB), "i"(GSPIN), "i"(D4), "i"(0)
        : "memory", "vcc", "scc", "s76", "s77", "s78", "s79", "s80", "s81", "s82", "s83");
      }
      TS(12);
      float x1[2][4], s1[2], s2[2];
      u32 h01[2], h23[2];
#pragma unroll
      for (int t = 0; t < 2; ++t) {
        const v4f ys = yt[t];
        h01[t] = cvtpk(ys[0] * gc[t][0] + xc[t][0], ys[1] * gc[t][1] + xc[t][1]); h23[t] = cvtpk(ys[2] * gc[t][2] + xc[t][2], ys[3] * gc[t][3] + xc[t][3]);
      }
      const v4u Ah = packA(h01[0], h23[0], h01[1], h23[1]);
      SCHED();
      CT(1);
      {
        v4f d[4];
        v4u A1, A2, A3;
        float u0, u1, sv;
#define MM(c, A, sl) d[c] = mfma(A, Bw[sl][c], sl ? d[c] : v4f{0, 0, 0, 0}); PIN(d[c]); SCHED()
#define X1(t, r) x1[t][r] = fmaf(yt[t][r], ga[t][r], xr[t][r])
        MM(0, Ah, 0); A1 = shl(Ah, IC<1>{}); PIN(A1); SCHED();
        MM(2, Ah, 0); A2 = shl(Ah, IC<2>{}); PIN(A2); SCHED();
        MM(0, A1, 1); A3 = shl(Ah, IC<3>{}); PIN(A3); SCHED();
        MM(2, A1, 1); X1(0, 0); X1(0, 1); X1(0, 2); X1(0, 3); PIN(x1[0][0]); PIN(x1[0][1]); PIN(x1[0][2]); PIN(x1[0][3]); SCHED();
        MM(0, A2, 2); X1(1, 0); X1(1, 1); X1(1, 2); X1(1, 3); PIN(x1[1][0]); PIN(x1[1][1]); PIN(x1[1][2]); PIN(x1[1][3]); SCHED();
        MM(2, A2, 2); s2[0] = x1[0][0] * x1[0][0]; s2[1] = x1[1][0] * x1[1][0]; s1[0] = x1[0][0] + x1[0][1]; s1[1] = x1[1][0] + x1[1][1]; PIN(s1[0]); PIN(s1[1]); PIN(s2[0]); PIN(s2[1]); SCHED();
        MM(0, A3, 3); s2[0] = fmaf(x1[0][1], x1[0][1], s2[0]); s2[1] = fmaf(x1[1][1], x1[1][1], s2[1]); u0 = x1[0][2] + x1[0][3]; u1 = x1[1][2] + x1[1][3]; PIN(u0); PIN(u1); PIN(s2[0]); PIN(s2[1]); SCHED();
        MM(2, A3, 3); s2[0] = fmaf(x1[0][2], x1[0][2], s2[0]); s2[1] = fmaf(x1[1][2], x1[1][2], s2[1]); s1[0] += u0; s1[1] += u1; PIN(s1[0]); PIN(s1[1]); PIN(s2[0]); PIN(s2[1]); SCHED();
        MM(1, Ah, 0); s2[0] = fmaf(x1[0][3], x1[0][3], s2[0]); s2[1] = fmaf(x1[1][3], x1[1][3], s2[1]); PIN(s2[0]); PIN(s2[1]); SCHED();
        MM(3, Ah, 0); swap32(s1[0], s2[0]); swap32(s1[1], s2[1]); s1[0] += s2[0]; s1[1] += s2[1]; PIN(s1[0]); PIN(s1[1]); SCHED();
        MM(1, A1, 1); swap16(s1[0], s1[1]); sv = s1[0] + s1[1]; PIN(sv); SCHED();
        MM(3, A1, 1); PIN(d[0]); PIN(d[2]); *(LAS float*)pw1 = d[0][0]; *(LAS float*)(pw1 + 8) = d[2][0]; CBAR(); sv += dppf<XOR1>(sv); PIN(sv); SCHED();
        MM(1, A2, 2); sv += dppf<XOR2>(sv); PIN(sv); SCHED();
        MM(3, A2, 2); sv += dppf<0x141>(sv); PIN(sv); SCHED();
        MM(1, A3, 3); *(LAS float*)pw0 = sv; CBAR(); SCHED();
        MM(3, A3, 3);
        *(LAS float*)(pw1 + 4) = d[1][0]; *(LAS float*)(pw1 + 12) = d[3][0];
      }
      CT(2);
      wgbar();
      CT(3);
      const v4f q0 = *(LAS v4f*)pr1, q1 = *(LAS v4f*)(pr1 + 16);
      v4f pg[4];
#pragma unroll
      for (int i = 0; i < 4; ++i) pg[i] = *(LAS v4f*)(pr0 + i * 1024);
      SCHED();
      const float S = (q0[0] + q0[2]) + (q1[0] + q1[2]), Q = (q0[1] + q0[3]) + (q1[1] + q1[3]);
      const float rstd = __builtin_amdgcn_rsqf(fmaxf(fmaf(S * S, -1.f / (512.f * 512.f), fmaf(Q, 1.f / 512.f, 1e-5f)), 1e-5f));
      v4f d = (pg[0] + pg[1]) + (pg[2] + pg[3]);
#pragma unroll
      for (int r = 0; r < 4; ++r) d[r] = fmaf(S, Cw[r], d[r]);
      const float e0_ = ex2(fmaf(d[0] * -LOG2E, rstd, Be[0])), e1_ = ex2(fmaf(d[1] * -LOG2E, rstd, Be[1]));
      const u32 ap = cvtpk(fmaf(d[0], rstd, Bq[0]) * fmaf(d[2], rstd, Bq[2]) * rcp(1.f + e0_), fmaf(d[1], rstd, Bq[1]) * fmaf(d[3], rstd, Bq[3]) * rcp(1.f + e1_));
      const v4u A2 = {ap, dppu<SHL + 1>(ap), dppu<SHL + 2>(ap), dppu<SHL + 3>(ap)};
      SCHED();
      CT(4);
      v4f z[8];
#pragma unroll
      for (int cc = 0; cc < 8; ++cc) { z[cc] = mfma(A2, Bd[cc], v4f{0, 0, 0, 0}); PIN(z[cc]); }
      SCHED();
      const v4u zw = {pkt(z[0][0], z[1][0], tw.x), pkt(z[2][0], z[3][0], tw.y), pkt(z[4][0], z[5][0], tw.z), pkt(z[6][0], z[7][0], tw.w)};
      st16(rs, zo, zw);
#pragma unroll
      for (int cc = 0; cc < 8; ++cc) asm volatile("" :: "v"(z[cc]));
      CT(5);
      asm volatile("s_waitcnt vmcnt(1)" :: "{v[160:191]}"(P[0]));
      TS(13);
      CTOUT(5);
      if (red) {
        const int hf = id & 1;
        v4u S = hf ? S1 : S0;
        float o[4], gb[4] = {blo(gbp.x), bhi(gbp.x), blo(gbp.y), bhi(gbp.y)};
#pragma unroll
        for (int r = 0; r < 4; ++r) { float u0 = x1[0][r], u1 = x1[1][r]; PIN(u0); PIN(u1); o[r] = (bt & 1) ? u1 : u0; PIN(o[r]); PIN(gb[r]); }
        rsrc_t ro = __builtin_amdgcn_make_buffer_rsrc(out, 0, 4096, 0x00020000);
        asm volatile("" : "+s"(ro));
        int oo = ((n >> 3) == (bt >> 1) && (g >> 1) == hf) ? (bt * 512 + fcol) * 2 : OOB;
        int zoff = WS_Z + (bt * 56 + (n >> 3) * 4 + g) * 1024 + (id & 7) * 128 + (nn & 7) * 16;
        PIN(S); PIN(oo); PIN(zoff);
        __builtin_amdgcn_s_sleep(D5);
        u32 w0, w1;
        asm volatile(
        "s_mov_b32 %2, %17\n\t1:\n\tbuffer_load_dwordx4 v[160:163], %8, %9, 0 offen sc1\n\tbuffer_load_dwordx4 v[164:167], %8, %9, %10 offen sc1\n\t"
        "buffer_load_dwordx4 v[168:171], %8, %9, %11 offen sc1\n\tbuffer_load_dwordx4 v[172:175], %8, %9, %12 offen sc1\n\t"
        "buffer_load_dwordx4 v[176:179], %8, %9, %13 offen sc1\n\tbuffer_load_dwordx4 v[180:183], %8, %9, %14 offen sc1\n\t"
        "buffer_load_dwordx4 v[184:187], %8, %9, %15 offen sc1\n\ts_sub_u32 %2, %2, 1\n\ts_waitcnt vmcnt(6)\n\tv_xor_b32 v160, v160, %4\n\tv_xor_b32 v161, v161, %5\n\t"
        "v_xor_b32 v162, v162, %6\n\tv_xor_b32 v163, v163, %7\n\tv_or3_b32 v192, v160, v161, v162\n\ts_nop 0\n\tv_mfma_f32_16x16x32_bf16 v[188:191], %3, v[160:163], 0\n\t"
        "s_waitcnt vmcnt(5)\n\tv_xor_b32 v164, v164, %4\n\tv_xor_b32 v165, v165, %5\n\tv_xor_b32 v166, v166, %6\n\tv_xor_b32 v167, v167, %7\n\tv_or3_b32 v192, v192, v163, v164\n\t"
        "v_or3_b32 v192, v192, v165, v166\n\tv_mfma_f32_16x16x32_bf16 v[188:191], %3, v[164:167], v[188:191]\n\ts_waitcnt vmcnt(4)\n\tv_xor_b32 v168, v168, %4\n\t"
        "v_xor_b32 v169, v169, %5\n\tv_xor_b32 v170, v170, %6\n\tv_xor_b32 v171, v171, %7\n\tv_or3_b32 v192, v192, v167, v168\n\tv_or3_b32 v192, v192, v169, v170\n\t"
        "v_mfma_f32_16x16x32_bf16 v[188:191], %3, v[168:171], v[188:191]\n\ts_waitcnt vmcnt(3)\n\tv_xor_b32 v172, v172, %4\n\tv_xor_b32 v173, v173, %5\n\t"
        "v_xor_b32 v174, v174, %6\n\tv_xor_b32 v175, v175, %7\n\tv_or3_b32 v192, v192, v171, v172\n\tv_or3_b32 v192, v192, v173, v174\n\t"
        "v_mfma_f32_16x16x32_bf16 v[188:191], %3, v[172:175], v[188:191]\n\ts_waitcnt vmcnt(2)\n\tv_xor_b32 v176, v176, %4\n\tv_xor_b32 v177, v177, %5\n\t"
        "v_xor_b32 v178, v178, %6\n\tv_xor_b32 v179, v179, %7\n\tv_or3_b32 v192, v192, v175, v176\n\tv_or3_b32 v192, v192, v177, v178\n\t"
        "v_mfma_f32_16x16x32_bf16 v[188:191], %3, v[176:179], v[188:191]\n\ts_waitcnt vmcnt(1)\n\tv_xor_b32 v180, v180, %4\n\tv_xor_b32 v181, v181, %5\n\t"
        "v_xor_b32 v182, v182, %6\n\tv_xor_b32 v183, v183, %7\n\tv_or3_b32 v192, v192, v179, v180\n\tv_or3_b32 v192, v192, v181, v182\n\t"
        "v_mfma_f32_16x16x32_bf16 v[188:191], %3, v[180:183], v[188:191]\n\ts_waitcnt vmcnt(0)\n\tv_xor_b32 v184, v184, %4\n\tv_xor_b32 v185, v185, %5\n\t"
        "v_xor_b32 v186, v186, %6\n\tv_xor_b32 v187, v187, %7\n\tv_or3_b32 v192, v192, v183, v184\n\tv_or3_b32 v192, v192, v185, v186\n\t"
        "v_mfma_f32_16x16x32_bf16 v[188:191], %3, v[184:187], v[188:191]\n\tv_bitop3_b32 v192, v192, %16, v187 bitop3:0xc8\n\tv_cmp_ne_u32_e32 vcc, 0, v192\n\t"
        "s_cbranch_scc1 2f\n\ts_cbranch_vccnz 1b\n\t2:\n\ts_nop 4\n\tv_fma_f32 v193, v188, %22, %18\n\tv_fma_f32 v194, v189, %23, %19\n\tv_fma_f32 v195, v190, %24, %20\n\t"
        "v_fma_f32 v196, v191, %25, %21\n\tv_fmac_f32_dpp v193, v188, %22 row_ror:8 row_mask:0xf bank_mask:0xf\n\t"
        "v_fmac_f32_dpp v194, v189, %23 row_ror:8 row_mask:0xf bank_mask:0xf\n\tv_fmac_f32_dpp v195, v190, %24 row_ror:8 row_mask:0xf bank_mask:0xf\n\t"
        "v_fmac_f32_dpp v196, v191, %25 row_ror:8 row_mask:0xf bank_mask:0xf\n\tv_cvt_pk_bf16_f32 %0, v193, v194\n\tv_cvt_pk_bf16_f32 %1, v195, v196\n\t"
        : "=&v"(w0), "=&v"(w1), "=&s"(cnt)
        : "v"(S), "v"(tw.x), "v"(tw.y), "v"(tw.z), "v"(tw.w), "v"(zoff), "s"(rd4), "s"(8192), "s"(16384), "s"(24576), "s"(32768), "s"(40960), "s"(49152), "s"(LSB), "i"(GSPIN),
          "v"(o[0]), "v"(o[1]), "v"(o[2]), "v"(o[3]), "v"(gb[0]), "v"(gb[1]), "v"(gb[2]), "v"(gb[3])
        : "memory", "vcc", "scc", "v160", "v161", "v162", "v163", "v164", "v165", "v166", "v167", "v168", "v169", "v170", "v171", "v172", "v173", "v174", "v175", "v176", "v177", "v178", "v179", "v180", "v181", "v182", "v183", "v184", "v185", "v186", "v187", "v188", "v189", "v190", "v191", "v192", "v193", "v194", "v195", "v196");
        __builtin_amdgcn_raw_buffer_store_b64(v2u{w0, w1}, ro, oo, 0, OAUX);
        TS(14);
      } else if (bid == 161 && a == 0) {
        st4(rs, lane == 0 ? WS_EPOCH : OOB, tag);
      }
    }
  }
  TS(15);
  if (CPF) asm volatile(".subsection 1\n.Lfk_end:\n\t.subsection 0");
}

#if defined(__HIP_DEVICE_COMPILE__) && !defined(PK)
#pragma clang attribute pop
#endif
extern "C" void kernel_launch(void* const* d_in, const int* in_sizes, int n_in, void* d_out, int out_size, void* d_ws, size_t ws_size,
                              hipStream_t stream) {
  fused_kernel<<<256, 512, 0, stream>>>((const u16*)d_in[0], (const u16*)d_in[9], (const u16*)d_in[3], (const u16*)d_in[10], (const u16*)d_in[1], (const u16*)d_in[2],
                                        (unsigned char*)d_ws, (const u16*)d_in[4], (const u16*)d_in[6], (const u16*)d_in[7], (const u16*)d_in[8], (u16*)d_out);
}
```
